```python
import torch

def _softmax(x, axis=-1):
    return torch.softmax(x, dim=axis)
def _gelu(x):
    return torch.nn.functional.gelu(x, approximate="tanh")
def _silu(x):
    return torch.nn.functional.silu(x)
def _sigmoid(x):
    return torch.sigmoid(x)
def _layernorm(x, axis=-1, eps=1e-5):
    m = x.mean(dim=axis, keepdim=True)
    v = x.var(dim=axis, keepdim=True, unbiased=False)
    return (x - m) / torch.sqrt(v + eps)
def _scatter_add(tgt, idx, src, axis=0):
    return tgt.clone().index_add_(axis, idx, src)
def _segment_sum(src, seg, n):
    out = torch.zeros((n,) + src.shape[1:], dtype=src.dtype)
    return out.index_add_(0, seg, src)
def _pad(x, pw):
    flat = tuple(v for pair in reversed(pw) for v in pair)
    return torch.nn.functional.pad(x, flat)

kernel_name = 'gather_scatter__edge_featured_mpnn_egc__graph'

def setup_inputs():

    _w = (lambda a: (lambda t: t.to(torch.bfloat16) if t.is_floating_point() else t)(torch.as_tensor(a)))
    N, deg, D, De = 25000, 32, 64, max(0, 1)
    E = N * deg
    return {
        "x": _w((lambda *s: torch.randn(*s, dtype=torch.bfloat16) * 0.2)(N, D)),
        "e_attr": _w((lambda *s: torch.randn(*s, dtype=torch.bfloat16) * 0.2)(E, De)),
        "src": _w((lambda lo, hi, *s: torch.randint(lo, hi, s))(0, N, E).to(torch.int64)),
        "dst": _w((lambda lo, hi, *s: torch.randint(lo, hi, s))(0, N, E).to(torch.int64)),
        "u_w": _w((lambda *s: torch.randn(*s, dtype=torch.bfloat16) * 0.05)(D, D)),
        "v_w": _w((lambda *s: torch.randn(*s, dtype=torch.bfloat16) * 0.05)(D, D)),
        "g_w": _w((lambda *s: torch.randn(*s, dtype=torch.bfloat16) * 0.2)(2 * D + De, D)),
    }

def reference(x, e_attr, src, dst, u_w, v_w, g_w):
    N, D = x.shape
    g_in = torch.concatenate([x[dst], x[src], e_attr], axis=-1)
    eta = _sigmoid(g_in @ g_w)
    hv = x @ v_w
    msg = eta * hv[src]
    agg = _segment_sum(msg, dst, N)
    return _silu(x @ u_w + agg)
```

```cpp
#include <hip/hip_runtime.h>
#include <hip/hip_bf16.h>
#include <stdint.h>

namespace {

typedef unsigned int u32;
typedef unsigned short u16;
typedef unsigned long long u64;
typedef u32 u32x2 __attribute__((ext_vector_type(2)));
typedef u32 u32x4 __attribute__((ext_vector_type(4)));
typedef int i32x4 __attribute__((ext_vector_type(4)));
typedef short s16x4 __attribute__((ext_vector_type(4)));
typedef short s16x8 __attribute__((ext_vector_type(8)));
typedef __bf16 bf16x8 __attribute__((ext_vector_type(8)));
typedef __bf16 bf16x2 __attribute__((ext_vector_type(2)));
typedef float f32x2 __attribute__((ext_vector_type(2)));
typedef float f32x16 __attribute__((ext_vector_type(16)));
#define LDS_AS __attribute__((address_space(3)))

constexpr int NN = 25000;
constexpr int NE = 800000;
constexpr int NCH = 256;
constexpr int CH = 3128;
constexpr int NWIN = 2048;
constexpr int OFFS_STRIDE = 2056;
constexpr float NLOG2E = -1.4426950408889634f;

constexpr size_t WS_REC = 0;
constexpr size_t WS_REC_BYTES = (size_t)NCH * CH * 8 + 4096;
constexpr size_t WS_OFFS = WS_REC + WS_REC_BYTES;
constexpr size_t WS_OFFS_BYTES = (size_t)NCH * OFFS_STRIDE * 2;
constexpr size_t WS_WF = WS_OFFS + WS_OFFS_BYTES;

__device__ __forceinline__ int win_of(int n) { return (int)(((u32)n * 256u) / 3125u); }
__device__ __forceinline__ int win_start(int w) { return (w * 3125 + 255) >> 8; }
__device__ __forceinline__ float bf2f(u32 b) { return __builtin_bit_cast(float, b << 16); }
__device__ __forceinline__ u32 pack_bf16(float lo, float hi) {
  f32x2 v = {lo, hi};
  return __builtin_bit_cast(u32, __builtin_convertvector(v, bf16x2));
}
template <int CTRL, int RMASK>
__device__ __forceinline__ u32 dpp_add(u32 v) {
  return v + (u32)__builtin_amdgcn_update_dpp(0, (int)v, CTRL, RMASK, 0xf, false);
}
__device__ __forceinline__ u32 scan32(u32 v) {
  v = dpp_add<0x111, 0xf>(v); v = dpp_add<0x112, 0xf>(v); v = dpp_add<0x114, 0xf>(v); v = dpp_add<0x118, 0xf>(v);
  v = dpp_add<0x142, 0xa>(v);
  return v;
}
__device__ __forceinline__ u32 scan64(u32 v) { v = scan32(v); return dpp_add<0x143, 0xc>(v); }

__global__ __launch_bounds__(1024) void k1_bucket(const int* __restrict__ src, const int* __restrict__ dst,
                                                  const u16* __restrict__ ea, const u16* __restrict__ gw,
                                                  const u16* __restrict__ vw, const u16* __restrict__ uw,
                                                  char* __restrict__ ws) {
  __shared__ u32 hist[NWIN];
  __shared__ u32 wsum[16];
  __shared__ __attribute__((aligned(16))) u64 stage[CH];
  const int tid = threadIdx.x, c = blockIdx.x, lane = tid & 63, wv = tid >> 6;
  const int e0 = c * CH + tid * 4;
  const bool act = tid < CH / 4 && e0 < NE;
  i32x4 d4 = {0, 0, 0, 0}, s4 = {0, 0, 0, 0};
  u32x2 e4 = {0, 0};
  if (act) {
    d4 = *(const i32x4*)(dst + e0);
    s4 = *(const i32x4*)(src + e0);
    e4 = *(const u32x2*)(ea + e0);
  }
  hist[2 * tid] = 0; hist[2 * tid + 1] = 0;
  if (c < 8 && tid >= 768) {
    const int id = c * 256 + (tid - 768);
    const int m = id >> 9, s = (id >> 7) & 3, t = (id >> 6) & 1, l = id & 63;
    const u16* W = m == 0 ? gw + 64 * 64 : (m == 1 ? vw : (m == 2 ? gw : uw));
    const int k0 = 16 * s + 8 * (l >> 5), f = 32 * t + (l & 31);
    float v[8];
#pragma unroll
    for (int j = 0; j < 8; ++j) { v[j] = bf2f(W[(k0 + j) * 64 + f]); if (m == 0) v[j] *= NLOG2E; }
    u32x4 o = {pack_bf16(v[0], v[1]), pack_bf16(v[2], v[3]), pack_bf16(v[4], v[5]), pack_bf16(v[6], v[7])};
    *(u32x4*)(ws + WS_WF + (size_t)id * 16) = o;
  }
  __syncthreads();
  int w[4]; u32 rk[4];
#pragma unroll
  for (int j = 0; j < 4; ++j) {
    w[j] = min(win_of(d4[j]), NWIN - 1);
    rk[j] = 0;
    if (act) rk[j] = atomicAdd(&hist[w[j]], 1u);
  }
  __syncthreads();
  const u32 h0 = hist[2 * tid], h1 = hist[2 * tid + 1];
  const u32 inc = scan64(h0 + h1);
  if (lane == 63) wsum[wv] = inc;
  __syncthreads();
  u32 pre = 0;
#pragma unroll
  for (int i = 0; i < 16; ++i) pre += i < wv ? wsum[i] : 0u;
  const u32 o0 = pre + inc - h0 - h1, o1 = o0 + h0;
  hist[2 * tid] = o0; hist[2 * tid + 1] = o1;
  u16* offs = (u16*)(ws + WS_OFFS) + (size_t)c * OFFS_STRIDE;
  *(u32*)(offs + 2 * tid) = o0 | (o1 << 16);
  if (tid == 1023) offs[NWIN] = (u16)(o1 + h1);
  __syncthreads();
  if (act) {
#pragma unroll
    for (int j = 0; j < 4; ++j) {
      const u32 d = (u32)(d4[j] - win_start(w[j])) & 15u;
      const u32 eb = (j & 1) ? (e4[j >> 1] & 0xffff0000u) : (e4[j >> 1] << 16);
      const u32 w1 = eb | (d << 5) | ((u32)(w[j] & 7) << 2);
      const u32 w0 = (u32)s4[j] << 7;
      stage[hist[w[j]] + rk[j]] = (u64)w0 | ((u64)w1 << 32);
    }
  }
  __syncthreads();
  u32x4* o4 = (u32x4*)(ws + WS_REC + (size_t)c * CH * 8);
  const u32x4* st4 = (const u32x4*)stage;
  o4[tid] = st4[tid];
  if (tid + 1024 < CH / 2) o4[tid + 1024] = st4[tid + 1024];
}

constexpr int CAPREC = 4096;
constexpr int L_LIST = 0;
constexpr int L_TRASH = L_LIST + CAPREC * 8;
constexpr int L_D = L_TRASH + 512;
constexpr int L_WTOT = L_D + NCH * 32;
constexpr int L_LUT = L_WTOT + 256;
constexpr int L_SCR = L_LUT + 512;
constexpr int L_RING = L_SCR + 8 * 1152;
constexpr int L_WST = L_RING + 8 * 8192;
constexpr int L_TOTAL = L_WST + 32768;
constexpr u32 NULL_W0 = 0xffffff00u, NULL_W1 = 15u << 5;

__device__ __forceinline__ void dma16(__amdgpu_buffer_rsrc_t rsrc, u32 voff, u32 ldsbase) {
  asm volatile("s_mov_b32 m0, %2\n\ts_nop 0\n\tbuffer_load_dwordx4 %0, %1, 0 offen lds"
               :: "v"(voff), "s"(rsrc), "s"(ldsbase) : "memory");
}
__device__ __forceinline__ f32x16 mfma(u32x4 a, u32x4 b, f32x16 c) {
  return __builtin_amdgcn_mfma_f32_32x32x16_bf16(__builtin_bit_cast(bf16x8, a), __builtin_bit_cast(bf16x8, b), c, 0, 0, 0);
}

__global__ __launch_bounds__(512, 2) void k2_main(const char* __restrict__ ws, const u16* __restrict__ x,
                                                  const u16* __restrict__ gw, u16* __restrict__ out) {
  __shared__ __attribute__((aligned(16))) char lds[L_TOTAL];
  const int tid = threadIdx.x, l = tid & 63, b = blockIdx.x;
  const int v = __builtin_amdgcn_readfirstlane(tid >> 6);
  const u32 lds0 = (u32)(uintptr_t)(LDS_AS char*)lds;
  const __amdgpu_buffer_rsrc_t rx = __builtin_amdgcn_make_buffer_rsrc((void*)x, 0, NN * 128, 0x00020000);
  const __amdgpu_buffer_rsrc_t rw = __builtin_amdgcn_make_buffer_rsrc((void*)(ws + WS_WF), 0, 32768, 0x00020000);
  const __amdgpu_buffer_rsrc_t rr = __builtin_amdgcn_make_buffer_rsrc((void*)(ws + WS_REC), 0, (u32)WS_REC_BYTES, 0x00020000);

  const int tseg = 32 * v + (l & 31);
  const u16* op = (const u16*)(ws + WS_OFFS) + (size_t)tseg * OFFS_STRIDE + 8 * b;
  const u32x4 o = *(const u32x4*)op;
  const u32 o8 = op[8];
#pragma unroll
  for (int i = 0; i < 4; ++i) dma16(rw, (u32)((4 * v + i) * 1024 + l * 16), lds0 + L_WST + (4 * v + i) * 1024);
  if (tid < 32) {
    const int d = tid >> 1, h = tid & 1;
    u32x4 q;
#pragma unroll
    for (int j2 = 0; j2 < 4; ++j2) {
      const int j = 2 * j2;
      const int n0 = (j & 3) + 8 * (j >> 2) + 4 * h;
      q[j2] = (d != 15 && n0 == d ? 0x3f80u : 0u) | (d != 15 && n0 + 1 == d ? 0x3f800000u : 0u);
    }
    *(u32x4*)(lds + L_LUT + tid * 16) = q;
  }
  const u32 start = o[0] & 0xffffu, len = o8 - start;
  const u32 sl = start | (len << 16);
  u32x4 ra[4], rb[4]; u32 st[4], ln[4];
  const int i8 = l & 7;
#pragma unroll
  for (int rho = 0; rho < 4; ++rho) {
    const u32 q = (u32)__builtin_amdgcn_ds_bpermute((8 * rho + (l >> 3)) * 4, (int)sl);
    st[rho] = q & 0xffffu; ln[rho] = q >> 16;
    const u32 t = 32 * v + 8 * rho + (l >> 3);
    const u32 a0 = (t * CH + st[rho] + 2 * i8) * 8;
    ra[rho] = __builtin_amdgcn_raw_buffer_load_b128(rr, 2u * i8 < ln[rho] ? a0 : 0xfffffff0u, 0, 0);
    rb[rho] = __builtin_amdgcn_raw_buffer_load_b128(rr, 16u + 2u * i8 < ln[rho] ? a0 + 128 : 0xfffffff0u, 0, 0);
  }
  u32 cn[4], sc[4];
  cn[0] = __builtin_amdgcn_alignbit(o[1], o[0], 16) - o[0];
  cn[1] = __builtin_amdgcn_alignbit(o[2], o[1], 16) - o[1];
  cn[2] = __builtin_amdgcn_alignbit(o[3], o[2], 16) - o[2];
  cn[3] = __builtin_amdgcn_alignbit(o8, o[3], 16) - o[3];
#pragma unroll
  for (int j = 0; j < 4; ++j) sc[j] = scan32(cn[j]);
  const u32 slen = scan32(len);
  if (l == 31) {
    *(u32x4*)(lds + L_WTOT + v * 32) = u32x4{sc[0], sc[1], sc[2], sc[3]};
    *(u32*)(lds + L_WTOT + v * 32 + 16) = slen;
  }
  asm volatile("s_waitcnt vmcnt(0)" ::: "memory");
  __syncthreads();
  u32 pre[4] = {0, 0, 0, 0}, tot[4] = {0, 0, 0, 0}, tlen = 0;
#pragma unroll
  for (int i = 0; i < 8; ++i) {
    const u32x4 q = *(const u32x4*)(lds + L_WTOT + i * 32);
    tlen += *(const u32*)(lds + L_WTOT + i * 32 + 16);
#pragma unroll
    for (int j = 0; j < 4; ++j) { tot[j] += q[j]; pre[j] += i < v ? q[j] : 0u; }
  }
  u32 T[8], B[8];
  {
    u32 acc = 0;
#pragma unroll
    for (int w = 0; w < 8; ++w) {
      const u32 q = __builtin_amdgcn_readfirstlane(tot[w >> 1]);
      T[w] = (w & 1) ? (q >> 16) : (q & 0xffffu);
      B[w] = acc; acc += (T[w] + 31u) & ~31u;
    }
  }
  if (l < 32) {
    int D[8];
#pragma unroll
    for (int w = 0; w < 8; ++w) {
      const u32 ex = pre[w >> 1] + sc[w >> 1] - cn[w >> 1];
      const u32 e = (w & 1) ? (ex >> 16) : (ex & 0xffffu);
      const u32 of = (w & 1) ? (o[w >> 1] >> 16) : (o[w >> 1] & 0xffffu);
      D[w] = (int)(B[w] + e - of) * 8;
    }
    *(i32x4*)(lds + L_D + tseg * 32) = i32x4{D[0], D[1], D[2], D[3]};
    *(i32x4*)(lds + L_D + tseg * 32 + 16) = i32x4{D[4], D[5], D[6], D[7]};
  }
  asm volatile("" ::: "memory");
  u32 Tv = T[0], Bv = B[0];
#pragma unroll
  for (int w = 1; w < 8; ++w) if (v == w) { Tv = T[w]; Bv = B[w]; }
  if (l < 32 && ((Tv + l) & ~31u) == (Tv & ~31u) && (Tv & 31u) != 0)
    *(u32x2*)(lds + L_LIST + (Bv + Tv + l) * 8) = u32x2{NULL_W0, NULL_W1};
#pragma unroll
  for (int rho = 0; rho < 4; ++rho) {
    const int t = 32 * v + 8 * rho + (l >> 3);
#pragma unroll
    for (int q = 0; q < 4; ++q) {
      const u32x4 r = q < 2 ? ra[rho] : rb[rho];
      const u32 w0 = (q & 1) ? r[2] : r[0], w1 = (q & 1) ? r[3] : r[1];
      const u32 rel = 2 * i8 + (q & 1) + (q >> 1) * 16;
      const int dd = *(const int*)(lds + L_D + t * 32 + (w1 & 0x1cu));
      const u32 pos = rel < ln[rho] ? (u32)(dd + (int)(st[rho] + rel) * 8) : (u32)(L_TRASH + l * 8);
      *(u32x2*)(lds + L_LIST + pos) = u32x2{w0, w1};
    }
    if (__builtin_amdgcn_ballot_w64(ln[rho] > 32u)) {
      for (u32 base = 32; __builtin_amdgcn_ballot_w64(ln[rho] > base); base += 8) {
        const u32 rel = base + i8;
        if (rel < ln[rho]) {
          const u32x2 r = *(const u32x2*)(ws + WS_REC + ((size_t)t * CH + st[rho] + rel) * 8);
          const int dd = *(const int*)(lds + L_D + t * 32 + (r[1] & 0x1cu));
          *(u32x2*)(lds + L_LIST + dd + (int)(st[rho] + rel) * 8) = r;
        }
      }
    }
  }
  const int r = l & 31, h = l >> 5;
  const int win = 8 * b + v;
  const int n0 = win_start(win), nn = win_start(win + 1) - n0;
  char* ring = lds + L_RING + v * 8192;
  const u32 ringa = lds0 + L_RING + v * 8192;
  const int gg = l >> 3, c8 = l & 7;
  const u32 swz0 = (u32)((c8 ^ (gg >> 1)) << 4), swz1 = swz0 ^ 64u;
#pragma unroll
  for (int i = 0; i < 4; ++i) {
    const u32 row = 8 * i + gg;
    dma16(rx, (i < 2 ? (u32)(n0 + row) * 128u : NULL_W0) + ((i & 1) ? swz1 : swz0), ringa + 4096 + i * 1024);
  }
  u32x4 WB[4][2], WV[4][2];
#pragma unroll
  for (int s = 0; s < 4; ++s)
#pragma unroll
    for (int t = 0; t < 2; ++t) {
      WB[s][t] = *(const u32x4*)(lds + L_WST + ((0 * 4 + s) * 2 + t) * 1024 + l * 16);
      WV[s][t] = *(const u32x4*)(lds + L_WST + ((1 * 4 + s) * 2 + t) * 1024 + l * 16);
    }
  const u32 aoff = r * 128 + ((h ^ ((r >> 1) & 7)) << 4);
  asm volatile("s_waitcnt vmcnt(0)" ::: "memory");
  f32x16 Y[2]; u32x4 PB[2];
  {
    u32x4 A[4];
#pragma unroll
    for (int s = 0; s < 4; ++s) A[s] = *(const u32x4*)(ring + 4096 + (aoff ^ (32 * s)));
#pragma unroll
    for (int t = 0; t < 2; ++t) {
      f32x16 p = {}, y = {};
#pragma unroll
      for (int s = 0; s < 4; ++s) {
        p = mfma(A[s], *(const u32x4*)(lds + L_WST + ((2 * 4 + s) * 2 + t) * 1024 + l * 16), p);
        y = mfma(A[s], *(const u32x4*)(lds + L_WST + ((3 * 4 + s) * 2 + t) * 1024 + l * 16), y);
      }
      Y[t] = y;
      float c = bf2f(gw[128 * 64 + 32 * t + r]);
      float p7 = h ? c : p[7];
      PB[t] = u32x4{pack_bf16(p[0] * NLOG2E, p[1] * NLOG2E), pack_bf16(p[2] * NLOG2E, p[3] * NLOG2E),
                    pack_bf16(p[4] * NLOG2E, p[5] * NLOG2E), pack_bf16(p[6] * NLOG2E, p7 * NLOG2E)};
    }
  }
  __syncthreads();

  const int ng = (int)((Tv + 31u) >> 5);
  const char* list = lds + L_LIST + Bv * 8;
  char* scr = lds + L_SCR + v * 1152;
  const u32 maskh = h ? 0xffff0000u : 0u;
  const int p4 = l & 3, q4 = (l >> 2) & 3;
  const u32 troff = (p4 & 1) * 576 + (p4 >> 1) * 8 + (4 * h + q4) * 16;
  auto gather = [&](int g) {
    const char* lp = list + g * 256 + gg * 8;
#pragma unroll
    for (int i = 0; i < 4; ++i) {
      const u32 w0 = *(const u32*)(lp + i * 64);
      dma16(rx, w0 + ((i & 1) ? swz1 : swz0), ringa + (g & 1) * 4096 + i * 1024);
    }
  };
  if (ng > 0) gather(0);
  for (int g = 0; g < ng; ++g) {
    if (g + 1 < ng) { gather(g + 1); asm volatile("s_waitcnt vmcnt(4)" ::: "memory"); }
    else asm volatile("s_waitcnt vmcnt(0)" ::: "memory");
    const char* slot = ring + (g & 1) * 4096;
    u32x4 A[4];
#pragma unroll
    for (int s = 0; s < 4; ++s) A[s] = *(const u32x4*)(slot + (aoff ^ (32 * s)));
    const u32 w1 = *(const u32*)(list + g * 256 + r * 8 + 4);
    u32x4 S = *(const u32x4*)(lds + L_LUT + (w1 & 0x1e0u) + h * 16);
    S[3] |= w1 & maskh;
    *(u32x4*)(scr + h * 576 + r * 16) = S;
    asm volatile("" ::: "memory");
    u32x4 ST[2];
#pragma unroll
    for (int s = 0; s < 2; ++s) {
      const s16x4 a = __builtin_amdgcn_ds_read_tr16_b64_v4i16((LDS_AS s16x4*)(scr + troff + s * 256));
      const s16x4 c = __builtin_amdgcn_ds_read_tr16_b64_v4i16((LDS_AS s16x4*)(scr + troff + s * 256 + 128));
      const u32x2 a2 = __builtin_bit_cast(u32x2, a), c2 = __builtin_bit_cast(u32x2, c);
      ST[s] = u32x4{a2[0], a2[1], c2[0], c2[1]};
    }
#pragma unroll
    for (int t = 0; t < 2; ++t) {
      f32x16 z = {}, hv = {};
#pragma unroll
      for (int s = 0; s < 4; ++s) z = mfma(A[s], WB[s][t], z);
      z = mfma(S, PB[t], z);
#pragma unroll
      for (int s = 0; s < 4; ++s) hv = mfma(A[s], WV[s][t], hv);
      u32x4 M[2];
#pragma unroll
      for (int j = 0; j < 8; ++j) {
        const float g0 = __builtin_amdgcn_rcpf(1.0f + __builtin_amdgcn_exp2f(z[2 * j]));
        const float g1 = __builtin_amdgcn_rcpf(1.0f + __builtin_amdgcn_exp2f(z[2 * j + 1]));
        M[j >> 2][j & 3] = pack_bf16(g0 * hv[2 * j], g1 * hv[2 * j + 1]);
      }
      Y[t] = mfma(ST[0], M[0], Y[t]);
      Y[t] = mfma(ST[1], M[1], Y[t]);
    }
  }
  u16* stg = (u16*)ring;
#pragma unroll
  for (int t = 0; t < 2; ++t)
#pragma unroll
    for (int j = 0; j < 8; ++j) {
      const float y = Y[t][j];
      const float s = y * __builtin_amdgcn_rcpf(1.0f + __builtin_amdgcn_exp2f(y * NLOG2E));
      const int row = (j & 3) + 8 * (j >> 2) + 4 * h;
      stg[row * 64 + 32 * t + r] = (u16)(pack_bf16(s, s) & 0xffffu);
    }
  asm volatile("" ::: "memory");
#pragma unroll
  for (int i = 0; i < 2; ++i) {
    const int ch = l + 64 * i;
    const u32x4 q = *(const u32x4*)((const char*)stg + ch * 16);
    if ((ch >> 3) < nn) *(u32x4*)((char*)out + (size_t)n0 * 128 + ch * 16) = q;
  }
}

}

extern "C" void kernel_launch(void* const* d_in, const int* in_sizes, int n_in, void* d_out, int out_size,
                              void* d_ws, size_t ws_size, hipStream_t stream) {
  const u16* x = (const u16*)d_in[0];
  const u16* ea = (const u16*)d_in[1];
  const int* src = (const int*)d_in[2];
  const int* dst = (const int*)d_in[3];
  const u16* uw = (const u16*)d_in[4];
  const u16* vw = (const u16*)d_in[5];
  const u16* gw = (const u16*)d_in[6];
  k1_bucket<<<NCH, 1024, 0, stream>>>(src, dst, ea, gw, vw, uw, (char*)d_ws);
  k2_main<<<NCH, 512, 0, stream>>>((const char*)d_ws, x, gw, (u16*)d_out);
}
```

```cpp
#include <hip/hip_runtime.h>
#include <hip/hip_bf16.h>
#include <stdint.h>

namespace {

typedef unsigned int u32;
typedef unsigned short u16;
typedef unsigned long long u64;
typedef u32 u32x2 __attribute__((ext_vector_type(2)));
typedef u32 u32x4 __attribute__((ext_vector_type(4)));
typedef __bf16 bf16x2 __attribute__((ext_vector_type(2)));
typedef float f32x2 __attribute__((ext_vector_type(2)));

constexpr int NN = 25000;
constexpr int NE = 800000;
constexpr int NCH = 256;
constexpr int CH = 3128;
constexpr int NWIN = 2048;
constexpr int OFFS_STRIDE = 2056;
constexpr float NLOG2E = -1.4426950408889634f;

constexpr size_t WS_REC = 0;
constexpr size_t WS_REC_BYTES = (size_t)NCH * CH * 8 + 4096;
constexpr size_t WS_OFFS = WS_REC + WS_REC_BYTES;
constexpr size_t WS_OFFS_BYTES = (size_t)NCH * OFFS_STRIDE * 2;
constexpr size_t WS_WF = WS_OFFS + WS_OFFS_BYTES;

__device__ __forceinline__ int win_start(int w) { return (w * 3125 + 255) >> 8; }
__device__ __forceinline__ float bf2f(u32 b) { return __builtin_bit_cast(float, b << 16); }
__device__ __forceinline__ u32 pack_bf16(float lo, float hi) {
  f32x2 v = {lo, hi};
  return __builtin_bit_cast(u32, __builtin_convertvector(v, bf16x2));
}
template <int CTRL, int RMASK>
__device__ __forceinline__ u32 dpp_add(u32 v) {
  return v + (u32)__builtin_amdgcn_update_dpp(0, (int)v, CTRL, RMASK, 0xf, false);
}
__device__ __forceinline__ u32 scan32(u32 v) {
  v = dpp_add<0x111, 0xf>(v); v = dpp_add<0x112, 0xf>(v); v = dpp_add<0x114, 0xf>(v); v = dpp_add<0x118, 0xf>(v);
  v = dpp_add<0x142, 0xa>(v);
  return v;
}
__device__ __forceinline__ u32 scan64(u32 v) { v = scan32(v); return dpp_add<0x143, 0xc>(v); }

constexpr int AUX_SC1 = 16;
constexpr int K2_CODE_LINES = 72;
constexpr int HROW = 8192;
__global__ __launch_bounds__(320) void k1_bucket(const int* __restrict__ src, const int* __restrict__ dst,
                                                 const u16* __restrict__ ea, const u16* __restrict__ gw,
                                                 const u16* __restrict__ vw, const u16* __restrict__ uw,
                                                 const u16* __restrict__ x, const u16* outp, char* __restrict__ ws) {
  __shared__ __attribute__((aligned(16))) char hist[3 * HROW + (NWIN + 64) * 2];
  __shared__ u32 wsum[4];
  __shared__ __attribute__((aligned(16))) u64 stage[CH + 64];
  const int tid = threadIdx.x, c = blockIdx.x, lane = tid & 63;
  const int wv = __builtin_amdgcn_readfirstlane(tid >> 6);
  const __amdgpu_buffer_rsrc_t rd = __builtin_amdgcn_make_buffer_rsrc((void*)dst, 0, NE * 4, 0x00020000);
  const __amdgpu_buffer_rsrc_t rs = __builtin_amdgcn_make_buffer_rsrc((void*)src, 0, NE * 4, 0x00020000);
  const __amdgpu_buffer_rsrc_t re = __builtin_amdgcn_make_buffer_rsrc((void*)ea, 0, NE * 2, 0x00020000);
  bool act[4]; u32 q[4];
#pragma unroll
  for (int k = 0; k < 4; ++k) {
    const u32 le = (tid + 256 * k) * 4, ge = c * CH + le;
    act[k] = tid < 256 && le < (u32)CH && ge < (u32)NE;
    q[k] = act[k] ? ge : 0x3fffff00u;
  }
  u32x4 d4[4], s4[4]; u32x2 e2[4];
  asm volatile("s_nop 4\n\tbuffer_load_dwordx4 %0, %12, %20, 0 offen nt\n\tbuffer_load_dwordx4 %1, %13, %20, 0 offen nt\n\t"
               "buffer_load_dwordx4 %2, %14, %20, 0 offen nt\n\tbuffer_load_dwordx4 %3, %15, %20, 0 offen nt\n\t"
               "buffer_load_dwordx4 %4, %12, %21, 0 offen nt\n\tbuffer_load_dwordx4 %5, %13, %21, 0 offen nt\n\t"
               "buffer_load_dwordx4 %6, %14, %21, 0 offen nt\n\tbuffer_load_dwordx4 %7, %15, %21, 0 offen nt\n\t"
               "buffer_load_dwordx2 %8, %16, %22, 0 offen nt\n\tbuffer_load_dwordx2 %9, %17, %22, 0 offen nt\n\t"
               "buffer_load_dwordx2 %10, %18, %22, 0 offen nt\n\tbuffer_load_dwordx2 %11, %19, %22, 0 offen nt"
               : "=&v"(d4[0]), "=&v"(d4[1]), "=&v"(d4[2]), "=&v"(d4[3]), "=&v"(s4[0]), "=&v"(s4[1]), "=&v"(s4[2]), "=&v"(s4[3]),
                 "=&v"(e2[0]), "=&v"(e2[1]), "=&v"(e2[2]), "=&v"(e2[3])
               : "v"(q[0] * 4), "v"(q[1] * 4), "v"(q[2] * 4), "v"(q[3] * 4), "v"(q[0] * 2), "v"(q[1] * 2), "v"(q[2] * 2), "v"(q[3] * 2),
                 "s"(rd), "s"(rs), "s"(re) : "memory");
  u32 pf0 = 0;
  if (wv < 4) {
#pragma unroll
    for (int i = 0; i < 4; ++i) *(u32x4*)(hist + wv * HROW + (lane + 64 * i) * 16) = u32x4{0, 0, 0, 0};
    if (lane < 8) *(u32x4*)(hist + wv * HROW + (lane + 256) * 16) = u32x4{0, 0, 0, 0};
  }
  const bool wprep = c < 32 && wv == 4;
  const int wid = c * 64 + lane, wm = wid >> 9;
  u32 wraw[8] = {0, 0, 0, 0, 0, 0, 0, 0};
  if (wprep) {
    const u16* W = wm == 0 ? gw + 64 * 64 : (wm == 1 ? vw : (wm == 2 ? gw : uw));
    const u16* wp = W + (16 * ((wid >> 7) & 3) + 8 * (lane >> 5)) * 64 + 32 * ((wid >> 6) & 1) + (lane & 31);
    asm volatile("global_load_ushort %0, %8, off\n\tglobal_load_ushort %1, %8, off offset:128\n\t"
                 "global_load_ushort %2, %8, off offset:256\n\tglobal_load_ushort %3, %8, off offset:384\n\t"
                 "global_load_ushort %4, %8, off offset:512\n\tglobal_load_ushort %5, %8, off offset:640\n\t"
                 "global_load_ushort %6, %8, off offset:768\n\tglobal_load_ushort %7, %8, off offset:896"
                 : "=&v"(wraw[0]), "=&v"(wraw[1]), "=&v"(wraw[2]), "=&v"(wraw[3]), "=&v"(wraw[4]), "=&v"(wraw[5]), "=&v"(wraw[6]), "=&v"(wraw[7])
                 : "v"(wp) : "memory");
  }
  u32 wn[16], ha[16], rk[16], w0[16], w1[16];
#pragma unroll
  for (int j = 0; j < 16; ++j)
    asm volatile("" : "=v"(wn[j]), "=v"(ha[j]), "=v"(rk[j]), "=v"(w0[j]), "=v"(w1[j]));
  asm volatile("s_waitcnt vmcnt(8)" : "+v"(d4[0]), "+v"(d4[1]), "+v"(d4[2]), "+v"(d4[3]) :: "memory");
  auto rank = [&](int j) {
    wn[j] = __umulhi(d4[j >> 2][j & 3], 351843721u);
    const u32 wd = act[j >> 2] ? wn[j] : (u32)(NWIN + lane);
    ha[j] = (wd << 1) | (u32)(wv * HROW);
    const u32 sh = (wd & 1u) << 4;
    rk[j] = __builtin_amdgcn_ubfe(atomicAdd((u32*)(hist + (ha[j] & ~3u)), 1u << sh), sh, 16u);
  };
  if (wv < 4) {
#pragma unroll
    for (int j = 0; j < 12; ++j) rank(j);
    if (wv == 0) {
#pragma unroll
      for (int j = 12; j < 16; ++j) rank(j);
    }
  }
  asm volatile("s_waitcnt vmcnt(0)" : "+v"(s4[0]), "+v"(s4[1]), "+v"(s4[2]), "+v"(s4[3]), "+v"(e2[0]), "+v"(e2[1]), "+v"(e2[2]), "+v"(e2[3]),
               "+v"(wraw[0]), "+v"(wraw[1]), "+v"(wraw[2]), "+v"(wraw[3]), "+v"(wraw[4]), "+v"(wraw[5]), "+v"(wraw[6]), "+v"(wraw[7]) :: "memory");
  auto record = [&](int j) {
    u32 rem;
    asm("v_mad_i32_i24 %0, %1, %2, %3" : "=v"(rem) : "v"(wn[j]), "s"(-3125), "v"(d4[j >> 2][j & 3] << 8));
    const u32 qe = e2[j >> 2][(j >> 1) & 1];
    const u32 eb = (j & 1) ? (qe & 0xffff0000u) : (qe << 16);
    w1[j] = (((rem >> 3) & 0x1e0u) | eb) | ((wn[j] & 7u) << 1);
    w0[j] = s4[j >> 2][j & 3] << 7;
    asm volatile("" : "+v"(w0[j]), "+v"(w1[j]));
  };
  if (wv < 4) {
#pragma unroll
    for (int j = 0; j < 12; ++j) record(j);
    if (wv == 0) {
#pragma unroll
      for (int j = 12; j < 16; ++j) record(j);
    }
  } else {
    if (wprep) {
      const __amdgpu_buffer_rsrc_t rwf = __builtin_amdgcn_make_buffer_rsrc((void*)(ws + WS_WF), 0, 32768, 0x00020000);
      float v[8];
#pragma unroll
      for (int j = 0; j < 8; ++j) v[j] = bf2f(wraw[j]) * (wm == 0 ? NLOG2E : 1.0f);
      const u32x4 o = {pack_bf16(v[0], v[1]), pack_bf16(v[2], v[3]), pack_bf16(v[4], v[5]), pack_bf16(v[6], v[7])};
      __builtin_amdgcn_raw_buffer_store_b128(o, rwf, wid * 16, 0, AUX_SC1);
    }
    u32 clo, chi;
    asm volatile("s_getpc_b64 s[40:41]\n\ts_add_u32 s40, s40, egc_k2_main@rel32@lo+4\n\ts_addc_u32 s41, s41, egc_k2_main@rel32@hi+12\n\t"
                 "s_mov_b32 %0, s40\n\ts_mov_b32 %1, s41" : "=s"(clo), "=s"(chi) :: "s40", "s41", "scc");
    const char* code = (const char*)(((u64)chi << 32) | clo);
    const int part = c >> 3;
    const char* p = code + min(part * 4 + ((lane - 50) & 3), K2_CODE_LINES - 1) * 128;
    if (lane < 25) p = (const char*)x + min((part * 25 + lane) * 4096, NN * 128 - 128);
    else if (lane < 50) p = (const char*)outp + min((part * 25 + lane - 25) * 4096, NN * 128 - 128);
    asm volatile("global_load_dword %0, %1, off" : "=&v"(pf0) : "v"(p) : "memory");
  }
  __syncthreads();
  u32x4 cw[4], tw = {0, 0, 0, 0};
  u32 loc = 0, inc = 0;
  if (wv < 4) {
#pragma unroll
    for (int k = 0; k < 4; ++k) { cw[k] = *(const u32x4*)(hist + k * HROW + tid * 16); tw += cw[k]; }
#pragma unroll
    for (int i = 0; i < 4; ++i) loc += (tw[i] & 0xffffu) + (tw[i] >> 16);
    inc = scan64(loc);
    if (lane == 63) wsum[wv] = inc;
  }
  __syncthreads();
  if (wv < 4) {
    u32 ws4 = wsum[lane & 3];
    ws4 = dpp_add<0x111, 0xf>(ws4); ws4 = dpp_add<0x112, 0xf>(ws4);
    u32 o = (wv ? (u32)__builtin_amdgcn_readlane((int)ws4, wv - 1) : 0u) + inc - loc;
    u32x4 pk;
#pragma unroll
    for (int i = 0; i < 4; ++i) {
      const u32 lo = o, hi = o + (tw[i] & 0xffffu);
      pk[i] = lo | (hi << 16);
      o = hi + (tw[i] >> 16);
    }
    const __amdgpu_buffer_rsrc_t rof = __builtin_amdgcn_make_buffer_rsrc((void*)(ws + WS_OFFS), 0, (u32)WS_OFFS_BYTES, 0x00020000);
    __builtin_amdgcn_raw_buffer_store_b128(pk, rof, (c * OFFS_STRIDE + 8 * tid) * 2, 0, AUX_SC1);
    if (tid == 255) __builtin_amdgcn_raw_buffer_store_b16((u16)o, rof, (c * OFFS_STRIDE + NWIN) * 2, 0, AUX_SC1);
#pragma unroll
    for (int k = 0; k < 4; ++k) { *(u32x4*)(hist + k * HROW + tid * 16) = pk; pk += cw[k]; }
  } else if (lane < 32) {
    constexpr u32 Z = CH | (CH << 16);
    *(u32x4*)(hist + (lane >> 3) * HROW + NWIN * 2 + (lane & 7) * 16) = u32x4{Z, Z, Z, Z};
  }
  __syncthreads();
  auto place = [&](int j0, int n) {
    u32 p[12];
#pragma unroll
    for (int j = 0; j < n; ++j) p[j] = *(const u16*)(hist + ha[j0 + j]);
#pragma unroll
    for (int j = 0; j < n; ++j) stage[min(p[j] + rk[j0 + j], (u32)CH + 63u)] = (u64)w0[j0 + j] | ((u64)w1[j0 + j] << 32);
  };
  if (wv < 4) {
    place(0, 12);
    if (wv == 0) place(12, 4);
  }
  __syncthreads();
  const __amdgpu_buffer_rsrc_t rrc = __builtin_amdgcn_make_buffer_rsrc((void*)(ws + WS_REC), 0, (u32)WS_REC_BYTES, 0x00020000);
  const u32x4* st4 = (const u32x4*)stage;
#pragma unroll
  for (int i = 0; i < 5; ++i) {
    const int k = tid + 320 * i;
    if (i < 4 || k < CH / 2) __builtin_amdgcn_raw_buffer_store_b128(st4[k], rrc, (c * CH * 8) + k * 16, 0, AUX_SC1);
  }
  asm volatile("s_waitcnt vmcnt(0)" : "+v"(pf0) :: "memory");
}

constexpr int CAPREC = 4096;
constexpr int L_LUT = 0;
constexpr int L_NULL = 512;
constexpr int L_ZERO = 768;
constexpr int L_WTOT = 1280;
constexpr int L_WST = 1536;
constexpr int L_D = L_WST + 32768;
constexpr int L_SCR = L_D + NCH * 16;
constexpr int L_LIST = L_SCR + 8 * 1152;
constexpr int L_RING = L_LIST + CAPREC * 8 + 512;
constexpr int L_OWN = L_RING + 8 * 8192;
constexpr int L_TOTAL = L_OWN + 8 * 2048;
constexpr u32 DBIAS = 3200;
constexpr u32 NULL_W0 = 0xffffff00u, NULL_W1 = 15u << 5;
typedef u32 u32x16 __attribute__((ext_vector_type(16)));
typedef float f32x32 __attribute__((ext_vector_type(32)));

#define K2_LOOP_ASM \
    "s_setprio 3\n\t" \
    "s_mov_b32 %[i], 0\n\t" \
    "s_add_u32 s72, %[ring], 0\n\t" \
    "s_add_u32 s73, %[ring], 1024\n\t" \
    "s_add_u32 s74, %[ring], 2048\n\t" \
    "s_add_u32 s75, %[ring], 3072\n\t" \
    "s_add_u32 s76, %[ring], 4096\n\t" \
    "s_add_u32 s77, %[ring], 5120\n\t" \
    "s_add_u32 s78, %[ring], 6144\n\t" \
    "s_add_u32 s79, %[ring], 7168\n\t" \
    "s_cmp_gt_u32 %[ng], 0\n\t" \
    "s_cselect_b32 %[t1], 0, %[nullrel]\n\t" \
    "v_add_u32 v238, %[t1], v20\n\t" \
    "ds_read2_b32 v[232:233], v238 offset0:0 offset1:16\n\t" \
    "ds_read2_b32 v[234:235], v238 offset0:32 offset1:48\n\t" \
    "s_add_u32 %[t0], %[i], 1\n\t" \
    "s_lshl_b32 %[t1], %[t0], 8\n\t" \
    "s_cmp_lt_u32 %[t0], %[ng]\n\t" \
    "s_cselect_b32 %[t1], %[t1], %[nullrel]\n\t" \
    "v_add_u32 v238, %[t1], v20\n\t" \
    "ds_read2_b32 v[228:229], v238 offset0:0 offset1:16\n\t" \
    "ds_read2_b32 v[230:231], v238 offset0:32 offset1:48\n\t" \
    "ds_read_b32 v236, v21\n\t" \
    "v_add_u32 v216, v16, v28\n\t" \
    "v_add_u32 v217, v17, v28\n\t" \
    "v_add_u32 v218, v18, v28\n\t" \
    "v_add_u32 v219, v19, v28\n\t" \
    "s_mov_b32 vcc_lo, 0xffff0000\n\t" \
    "s_mov_b32 vcc_hi, 0xffff0000\n\t" \
    "v_mov_b32 v237, %[zero]\n\t" \
    "v_cndmask_b32 v216, v216, v237, vcc\n\t" \
    "v_cndmask_b32 v217, v217, v237, vcc\n\t" \
    "v_cndmask_b32 v218, v218, v237, vcc\n\t" \
    "v_cndmask_b32 v219, v219, v237, vcc\n\t" \
    "ds_read_b128 v[200:203], v216\n\t" \
    "ds_read_b128 v[204:207], v217\n\t" \
    "ds_read_b128 v[208:211], v218\n\t" \
    "ds_read_b128 v[212:215], v219\n\t" \
    "s_waitcnt lgkmcnt(7)\n\t" \
    "v_add_u32 v232, v232, v22\n\t" \
    "v_add_u32 v233, v233, v23\n\t" \
    "v_add_u32 v234, v234, v22\n\t" \
    "v_add_u32 v235, v235, v23\n\t" \
    "s_add_u32 m0, %[ring], 0\n\t" \
    "s_nop 0\n\t" \
    "buffer_load_dwordx4 v232, %[rx], 0 offen lds\n\t" \
    "s_add_u32 m0, %[ring], 1024\n\t" \
    "s_nop 0\n\t" \
    "buffer_load_dwordx4 v233, %[rx], 0 offen lds\n\t" \
    "s_add_u32 m0, %[ring], 2048\n\t" \
    "s_nop 0\n\t" \
    "buffer_load_dwordx4 v234, %[rx], 0 offen lds\n\t" \
    "s_add_u32 m0, %[ring], 3072\n\t" \
    "s_nop 0\n\t" \
    "buffer_load_dwordx4 v235, %[rx], 0 offen lds\n\t" \
    "s_waitcnt lgkmcnt(5)\n\t" \
    "v_add_u32 v228, v228, v22\n\t" \
    "v_add_u32 v229, v229, v23\n\t" \
    "v_add_u32 v230, v230, v22\n\t" \
    "v_add_u32 v231, v231, v23\n\t" \
    "s_waitcnt lgkmcnt(4)\n\t" \
    "v_and_or_b32 v237, v236, %[m1e0], v24\n\t" \
    "s_cmp_eq_u32 %[first], 0\n\t" \
    "s_cbranch_scc1 NOTFIRST%=\n\t" \
    "ds_read_b128 v[168:171], v29 offset:24576\n\t" \
    "ds_read_b128 v[172:175], v29 offset:25600\n\t" \
    "ds_read_b128 v[176:179], v29 offset:26624\n\t" \
    "ds_read_b128 v[180:183], v29 offset:27648\n\t" \
    "ds_read_b128 v[184:187], v29 offset:28672\n\t" \
    "ds_read_b128 v[188:191], v29 offset:29696\n\t" \
    "ds_read_b128 v[192:195], v29 offset:30720\n\t" \
    "ds_read_b128 v[196:199], v29 offset:31744\n\t" \
    "s_waitcnt lgkmcnt(8)\n\t" \
    "ds_read_b128 v[216:219], v237 offset:%[lut]\n\t" \
    "ds_read_b128 v[136:139], v29 offset:16384\n\t" \
    "ds_read_b128 v[140:143], v29 offset:17408\n\t" \
    "s_waitcnt lgkmcnt(9)\n\t" \
    "v_mfma_f32_32x32x16_bf16 v[104:119], v[200:203], v[168:171], 0\n\t" \
    "v_mfma_f32_32x32x16_bf16 v[120:135], v[200:203], v[172:175], 0\n\t" \
    "ds_read_b128 v[144:147], v29 offset:18432\n\t" \
    "ds_read_b128 v[148:151], v29 offset:19456\n\t" \
    "s_waitcnt lgkmcnt(9)\n\t" \
    "v_mfma_f32_32x32x16_bf16 v[104:119], v[204:207], v[176:179], v[104:119]\n\t" \
    "v_mfma_f32_32x32x16_bf16 v[120:135], v[204:207], v[180:183], v[120:135]\n\t" \
    "ds_read_b128 v[152:155], v29 offset:20480\n\t" \
    "ds_read_b128 v[156:159], v29 offset:21504\n\t" \
    "s_waitcnt lgkmcnt(9)\n\t" \
    "v_mfma_f32_32x32x16_bf16 v[104:119], v[208:211], v[184:187], v[104:119]\n\t" \
    "v_mfma_f32_32x32x16_bf16 v[120:135], v[208:211], v[188:191], v[120:135]\n\t" \
    "ds_read_b128 v[160:163], v29 offset:22528\n\t" \
    "ds_read_b128 v[164:167], v29 offset:23552\n\t" \
    "s_waitcnt lgkmcnt(9)\n\t" \
    "v_mfma_f32_32x32x16_bf16 v[104:119], v[212:215], v[192:195], v[104:119]\n\t" \
    "v_mfma_f32_32x32x16_bf16 v[120:135], v[212:215], v[196:199], v[120:135]\n\t" \
    "ds_read_b128 v[32:35], v29 offset:0\n\t" \
    "ds_read_b128 v[36:39], v29 offset:1024\n\t" \
    "s_waitcnt lgkmcnt(8)\n\t" \
    "v_mfma_f32_32x32x16_bf16 v[168:183], v[200:203], v[136:139], 0\n\t" \
    "v_mfma_f32_32x32x16_bf16 v[184:199], v[200:203], v[140:143], 0\n\t" \
    "ds_read_b128 v[40:43], v29 offset:2048\n\t" \
    "ds_read_b128 v[44:47], v29 offset:3072\n\t" \
    "s_waitcnt lgkmcnt(8)\n\t" \
    "v_mfma_f32_32x32x16_bf16 v[168:183], v[204:207], v[144:147], v[168:183]\n\t" \
    "v_mfma_f32_32x32x16_bf16 v[184:199], v[204:207], v[148:151], v[184:199]\n\t" \
    "ds_read_b128 v[48:51], v29 offset:4096\n\t" \
    "ds_read_b128 v[52:55], v29 offset:5120\n\t" \
    "s_waitcnt lgkmcnt(8)\n\t" \
    "v_mfma_f32_32x32x16_bf16 v[168:183], v[208:211], v[152:155], v[168:183]\n\t" \
    "v_mfma_f32_32x32x16_bf16 v[184:199], v[208:211], v[156:159], v[184:199]\n\t" \
    "ds_read_b128 v[56:59], v29 offset:6144\n\t" \
    "ds_read_b128 v[60:63], v29 offset:7168\n\t" \
    "s_waitcnt lgkmcnt(8)\n\t" \
    "v_mfma_f32_32x32x16_bf16 v[168:183], v[212:215], v[160:163], v[168:183]\n\t" \
    "v_mfma_f32_32x32x16_bf16 v[184:199], v[212:215], v[164:167], v[184:199]\n\t" \
    "JOIN%=:\n\t" \
    "v_and_or_b32 v219, v236, v25, v219\n\t" \
    "ds_write_b128 v26, v[216:219]\n\t" \
    "s_add_u32 m0, %[ring], 4096\n\t" \
    "s_nop 0\n\t" \
    "buffer_load_dwordx4 v228, %[rx], 0 offen lds\n\t" \
    "s_add_u32 m0, %[ring], 5120\n\t" \
    "s_nop 0\n\t" \
    "buffer_load_dwordx4 v229, %[rx], 0 offen lds\n\t" \
    "s_add_u32 m0, %[ring], 6144\n\t" \
    "s_nop 0\n\t" \
    "buffer_load_dwordx4 v230, %[rx], 0 offen lds\n\t" \
    "s_add_u32 m0, %[ring], 7168\n\t" \
    "s_nop 0\n\t" \
    "buffer_load_dwordx4 v231, %[rx], 0 offen lds\n\t" \
    "s_waitcnt lgkmcnt(5)\n\t" \
    "ds_read_b128 v[64:67], v29 offset:8192\n\t" \
    "ds_read_b128 v[68:71], v29 offset:9216\n\t" \
    "ds_read_b128 v[72:75], v29 offset:10240\n\t" \
    "ds_read_b128 v[76:79], v29 offset:11264\n\t" \
    "ds_read_b128 v[80:83], v29 offset:12288\n\t" \
    "ds_read_b128 v[84:87], v29 offset:13312\n\t" \
    "ds_read_b128 v[88:91], v29 offset:14336\n\t" \
    "ds_read_b128 v[92:95], v29 offset:15360\n\t" \
    "s_add_u32 %[t0], %[i], 2\n\t" \
    "s_lshl_b32 %[t1], %[t0], 8\n\t" \
    "s_cmp_lt_u32 %[t0], %[ng]\n\t" \
    "s_cselect_b32 %[t1], %[t1], %[nullrel]\n\t" \
    "v_add_u32 v238, %[t1], v20\n\t" \
    "s_nop 7\n\t" \
    "s_nop 3\n\t" \
    "v_mul_f32 v168, %[nl2e], v168\n\t" \
    "v_mul_f32 v169, %[nl2e], v169\n\t" \
    "v_mul_f32 v170, %[nl2e], v170\n\t" \
    "v_mul_f32 v171, %[nl2e], v171\n\t" \
    "v_mul_f32 v172, %[nl2e], v172\n\t" \
    "v_mul_f32 v173, %[nl2e], v173\n\t" \
    "v_mul_f32 v174, %[nl2e], v174\n\t" \
    "v_mul_f32 v175, %[nl2e], v175\n\t" \
    "v_cvt_pk_bf16_f32 v96, v168, v169\n\t" \
    "v_cvt_pk_bf16_f32 v97, v170, v171\n\t" \
    "v_cvt_pk_bf16_f32 v98, v172, v173\n\t" \
    "v_cvt_pk_bf16_f32 v99, v174, v175\n\t" \
    "v_bfi_b32 v99, v25, v30, v99\n\t" \
    "v_mul_f32 v184, %[nl2e], v184\n\t" \
    "v_mul_f32 v185, %[nl2e], v185\n\t" \
    "v_mul_f32 v186, %[nl2e], v186\n\t" \
    "v_mul_f32 v187, %[nl2e], v187\n\t" \
    "v_mul_f32 v188, %[nl2e], v188\n\t" \
    "v_mul_f32 v189, %[nl2e], v189\n\t" \
    "v_mul_f32 v190, %[nl2e], v190\n\t" \
    "v_mul_f32 v191, %[nl2e], v191\n\t" \
    "v_cvt_pk_bf16_f32 v100, v184, v185\n\t" \
    "v_cvt_pk_bf16_f32 v101, v186, v187\n\t" \
    "v_cvt_pk_bf16_f32 v102, v188, v189\n\t" \
    "v_cvt_pk_bf16_f32 v103, v190, v191\n\t" \
    "v_bfi_b32 v103, v25, v31, v103\n\t" \
    "s_cmp_eq_u32 %[ng], 0\n\t" \
    "s_cbranch_scc1 END%=\n\t" \
    "s_waitcnt vmcnt(4) lgkmcnt(0)\n\t" \
    "ds_read_b128 v[200:203], v16 offset:0\n\t" \
    "ds_read_b128 v[204:207], v17 offset:0\n\t" \
    "ds_read_b128 v[208:211], v18 offset:0\n\t" \
    "ds_read_b128 v[212:215], v19 offset:0\n\t" \
    "s_waitcnt lgkmcnt(3)\n\t" \
    "v_mfma_f32_32x32x16_bf16 v[136:151], v[200:203], v[32:35], 0\n\t" \
    "s_waitcnt lgkmcnt(2)\n\t" \
    "v_mfma_f32_32x32x16_bf16 v[136:151], v[204:207], v[40:43], v[136:151]\n\t" \
    "s_waitcnt lgkmcnt(1)\n\t" \
    "v_mfma_f32_32x32x16_bf16 v[136:151], v[208:211], v[48:51], v[136:151]\n\t" \
    "s_waitcnt lgkmcnt(0)\n\t" \
    "v_mfma_f32_32x32x16_bf16 v[136:151], v[212:215], v[56:59], v[136:151]\n\t" \
    "v_mfma_f32_32x32x16_bf16 v[136:151], v[216:219], v[96:99], v[136:151]\n\t" \
    "s_nop 7\n\t" \
    "s_nop 7\n\t" \
    "s_setprio 1\n\t" \
    "s_lshl_b32 s80, %[ng], 8\n\t" \
    "s_add_u32 s81, s80, 0x200\n\t" \
    "s_movk_i32 %[i], 0x300\n\t" \
    "v_exp_f32 v136, v136\n\t" \
    "ds_read2_b32 v[232:233], v238 offset0:0 offset1:16\n\t" \
    "v_exp_f32 v137, v137\n\t" \
    "ds_read2_b32 v[234:235], v238 offset0:32 offset1:48\n\t" \
    "v_mfma_f32_32x32x16_bf16 v[168:183], v[200:203], v[64:67], 0\n\t" \
    "v_exp_f32 v138, v138\n\t" \
    "ds_read_b32 v236, v21 offset:256\n\t" \
    "v_exp_f32 v139, v139\n\t" \
    "ds_read_b64_tr_b16 v[220:221], v27 offset:0\n\t" \
    "v_mfma_f32_32x32x16_bf16 v[168:183], v[204:207], v[72:75], v[168:183]\n\t" \
    "v_exp_f32 v140, v140\n\t" \
    "ds_read_b64_tr_b16 v[222:223], v27 offset:128\n\t" \
    "v_exp_f32 v141, v141\n\t" \
    "v_mfma_f32_32x32x16_bf16 v[168:183], v[208:211], v[80:83], v[168:183]\n\t" \
    "v_exp_f32 v142, v142\n\t" \
    "s_waitcnt lgkmcnt(2)\n\t" \
    "v_exp_f32 v143, v143\n\t" \
    "s_mov_b32 m0, s72\n\t" \
    "v_mfma_f32_32x32x16_bf16 v[168:183], v[212:215], v[88:91], v[168:183]\n\t" \
    "s_setprio 0\n\t" \
    "v_add_f32 v136, 1.0, v136\n\t" \
    "v_add_f32 v137, 1.0, v137\n\t" \
    "v_add_f32 v138, 1.0, v138\n\t" \
    "v_add_f32 v139, 1.0, v139\n\t" \
    "v_add_f32 v140, 1.0, v140\n\t" \
    "v_add_f32 v141, 1.0, v141\n\t" \
    "v_add_f32 v142, 1.0, v142\n\t" \
    "v_add_f32 v143, 1.0, v143\n\t" \
    "v_add_u32 v232, v232, v22\n\t" \
    "v_add_u32 v233, v233, v23\n\t" \
    "v_add_u32 v234, v234, v22\n\t" \
    "v_add_u32 v235, v235, v23\n\t" \
    "buffer_load_dwordx4 v232, %[rx], 0 offen lds\n\t" \
    "s_setprio 1\n\t" \
    "v_exp_f32 v144, v144\n\t" \
    "v_rcp_f32 v136, v136\n\t" \
    "v_exp_f32 v145, v145\n\t" \
    "v_rcp_f32 v137, v137\n\t" \
    "v_mfma_f32_32x32x16_bf16 v[152:167], v[200:203], v[36:39], 0\n\t" \
    "v_exp_f32 v146, v146\n\t" \
    "ds_read_b64_tr_b16 v[224:225], v27 offset:256\n\t" \
    "v_rcp_f32 v138, v138\n\t" \
    "ds_read_b64_tr_b16 v[226:227], v27 offset:384\n\t" \
    "v_exp_f32 v147, v147\n\t" \
    "v_rcp_f32 v139, v139\n\t" \
    "v_mfma_f32_32x32x16_bf16 v[152:167], v[204:207], v[44:47], v[152:167]\n\t" \
    "v_exp_f32 v148, v148\n\t" \
    "v_rcp_f32 v140, v140\n\t" \
    "v_exp_f32 v149, v149\n\t" \
    "v_rcp_f32 v141, v141\n\t" \
    "v_mfma_f32_32x32x16_bf16 v[152:167], v[208:211], v[52:55], v[152:167]\n\t" \
    "v_exp_f32 v150, v150\n\t" \
    "s_cmp_ge_u32 %[i], s81\n\t" \
    "v_rcp_f32 v142, v142\n\t" \
    "v_exp_f32 v151, v151\n\t" \
    "s_mov_b32 m0, s73\n\t" \
    "v_rcp_f32 v143, v143\n\t" \
    "v_mfma_f32_32x32x16_bf16 v[152:167], v[212:215], v[60:63], v[152:167]\n\t" \
    "s_setprio 0\n\t" \
    "v_add_f32 v144, 1.0, v144\n\t" \
    "v_mul_f32 v168, v136, v168\n\t" \
    "v_add_f32 v145, 1.0, v145\n\t" \
    "v_mul_f32 v169, v137, v169\n\t" \
    "v_mfma_f32_32x32x16_bf16 v[152:167], v[216:219], v[100:103], v[152:167]\n\t" \
    "v_add_f32 v146, 1.0, v146\n\t" \
    "v_mul_f32 v170, v138, v170\n\t" \
    "v_add_f32 v147, 1.0, v147\n\t" \
    "v_mul_f32 v171, v139, v171\n\t" \
    "v_add_f32 v148, 1.0, v148\n\t" \
    "v_mul_f32 v172, v140, v172\n\t" \
    "v_add_f32 v149, 1.0, v149\n\t" \
    "v_mul_f32 v173, v141, v173\n\t" \
    "v_add_f32 v150, 1.0, v150\n\t" \
    "v_mul_f32 v174, v142, v174\n\t" \
    "v_add_f32 v151, 1.0, v151\n\t" \
    "v_mul_f32 v175, v143, v175\n\t" \
    "buffer_load_dwordx4 v233, %[rx], 0 offen lds\n\t" \
    "s_setprio 1\n\t" \
    "s_branch MID%=\n\t" \
    "LOOP%=:\n\t" \
    "v_exp_f32 v136, v136\n\t" \
    "ds_read2_b32 v[232:233], v238 offset0:0 offset1:16\n\t" \
    "v_rcp_f32 v160, v160\n\t" \
    "ds_read2_b32 v[234:235], v238 offset0:32 offset1:48\n\t" \
    "v_exp_f32 v137, v137\n\t" \
    "ds_read_b32 v236, v21 offset:256\n\t" \
    "v_mfma_f32_32x32x16_bf16 v[168:183], v[200:203], v[64:67], 0\n\t" \
    "v_rcp_f32 v161, v161\n\t" \
    "v_cvt_pk_bf16_f32 v228, v184, v185\n\t" \
    "v_cvt_pk_bf16_f32 v229, v186, v187\n\t" \
    "v_exp_f32 v138, v138\n\t" \
    "v_mfma_f32_32x32x16_bf16 v[168:183], v[204:207], v[72:75], v[168:183]\n\t" \
    "v_rcp_f32 v162, v162\n\t" \
    "v_cvt_pk_bf16_f32 v230, v188, v189\n\t" \
    "v_cvt_pk_bf16_f32 v231, v190, v191\n\t" \
    "v_exp_f32 v139, v139\n\t" \
    "v_mfma_f32_32x32x16_bf16 v[168:183], v[208:211], v[80:83], v[168:183]\n\t" \
    "v_rcp_f32 v163, v163\n\t" \
    "v_exp_f32 v140, v140\n\t" \
    "v_rcp_f32 v164, v164\n\t" \
    "v_mfma_f32_32x32x16_bf16 v[120:135], v[220:223], v[228:231], v[120:135]\n\t" \
    "v_exp_f32 v141, v141\n\t" \
    "ds_read_b64_tr_b16 v[220:221], v27 offset:0\n\t" \
    "v_rcp_f32 v165, v165\n\t" \
    "ds_read_b64_tr_b16 v[222:223], v27 offset:128\n\t" \
    "v_exp_f32 v142, v142\n\t" \
    "s_waitcnt lgkmcnt(2)\n\t" \
    "v_mfma_f32_32x32x16_bf16 v[168:183], v[212:215], v[88:91], v[168:183]\n\t" \
    "v_rcp_f32 v166, v166\n\t" \
    "v_exp_f32 v143, v143\n\t" \
    "s_mov_b32 m0, s72\n\t" \
    "v_rcp_f32 v167, v167\n\t" \
    "s_setprio 0\n\t" \
    "v_add_f32 v136, 1.0, v136\n\t" \
    "v_mul_f32 v192, v160, v192\n\t" \
    "v_add_f32 v137, 1.0, v137\n\t" \
    "v_mul_f32 v193, v161, v193\n\t" \
    "v_add_f32 v138, 1.0, v138\n\t" \
    "v_mul_f32 v194, v162, v194\n\t" \
    "v_add_f32 v139, 1.0, v139\n\t" \
    "v_mul_f32 v195, v163, v195\n\t" \
    "v_add_f32 v140, 1.0, v140\n\t" \
    "v_mul_f32 v196, v164, v196\n\t" \
    "v_add_f32 v141, 1.0, v141\n\t" \
    "v_mul_f32 v197, v165, v197\n\t" \
    "v_add_f32 v142, 1.0, v142\n\t" \
    "v_mul_f32 v198, v166, v198\n\t" \
    "v_add_f32 v143, 1.0, v143\n\t" \
    "v_mul_f32 v199, v167, v199\n\t" \
    "v_add_u32 v232, v232, v22\n\t" \
    "v_add_u32 v233, v233, v23\n\t" \
    "v_add_u32 v234, v234, v22\n\t" \
    "v_add_u32 v235, v235, v23\n\t" \
    "buffer_load_dwordx4 v232, %[rx], 0 offen lds\n\t" \
    "s_setprio 1\n\t" \
    "v_exp_f32 v144, v144\n\t" \
    "v_rcp_f32 v136, v136\n\t" \
    "v_exp_f32 v145, v145\n\t" \
    "v_mfma_f32_32x32x16_bf16 v[152:167], v[200:203], v[36:39], 0\n\t" \
    "v_rcp_f32 v137, v137\n\t" \
    "v_cvt_pk_bf16_f32 v228, v192, v193\n\t" \
    "v_cvt_pk_bf16_f32 v229, v194, v195\n\t" \
    "v_exp_f32 v146, v146\n\t" \
    "v_mfma_f32_32x32x16_bf16 v[152:167], v[204:207], v[44:47], v[152:167]\n\t" \
    "v_rcp_f32 v138, v138\n\t" \
    "v_cvt_pk_bf16_f32 v230, v196, v197\n\t" \
    "v_cvt_pk_bf16_f32 v231, v198, v199\n\t" \
    "v_exp_f32 v147, v147\n\t" \
    "v_mfma_f32_32x32x16_bf16 v[152:167], v[208:211], v[52:55], v[152:167]\n\t" \
    "v_rcp_f32 v139, v139\n\t" \
    "v_exp_f32 v148, v148\n\t" \
    "v_rcp_f32 v140, v140\n\t" \
    "v_mfma_f32_32x32x16_bf16 v[120:135], v[224:227], v[228:231], v[120:135]\n\t" \
    "v_exp_f32 v149, v149\n\t" \
    "ds_read_b64_tr_b16 v[224:225], v27 offset:256\n\t" \
    "v_rcp_f32 v141, v141\n\t" \
    "ds_read_b64_tr_b16 v[226:227], v27 offset:384\n\t" \
    "v_exp_f32 v150, v150\n\t" \
    "v_mfma_f32_32x32x16_bf16 v[152:167], v[212:215], v[60:63], v[152:167]\n\t" \
    "v_rcp_f32 v142, v142\n\t" \
    "s_cmp_ge_u32 %[i], s81\n\t" \
    "v_exp_f32 v151, v151\n\t" \
    "s_mov_b32 m0, s73\n\t" \
    "v_rcp_f32 v143, v143\n\t" \
    "s_setprio 0\n\t" \
    "v_add_f32 v144, 1.0, v144\n\t" \
    "v_mul_f32 v168, v136, v168\n\t" \
    "v_add_f32 v145, 1.0, v145\n\t" \
    "v_mul_f32 v169, v137, v169\n\t" \
    "v_mfma_f32_32x32x16_bf16 v[152:167], v[216:219], v[100:103], v[152:167]\n\t" \
    "v_add_f32 v146, 1.0, v146\n\t" \
    "v_mul_f32 v170, v138, v170\n\t" \
    "v_add_f32 v147, 1.0, v147\n\t" \
    "v_mul_f32 v171, v139, v171\n\t" \
    "v_add_f32 v148, 1.0, v148\n\t" \
    "v_mul_f32 v172, v140, v172\n\t" \
    "v_add_f32 v149, 1.0, v149\n\t" \
    "v_mul_f32 v173, v141, v173\n\t" \
    "v_add_f32 v150, 1.0, v150\n\t" \
    "v_mul_f32 v174, v142, v174\n\t" \
    "v_add_f32 v151, 1.0, v151\n\t" \
    "v_mul_f32 v175, v143, v175\n\t" \
    "buffer_load_dwordx4 v233, %[rx], 0 offen lds\n\t" \
    "s_setprio 1\n\t" \
    "MID%=:\n\t" \
    "s_cbranch_scc1 LAST%=\n\t" \
    "v_and_or_b32 v237, v236, %[m1e0], v24\n\t" \
    "ds_read_b128 v[216:219], v237 offset:%[lut]\n\t" \
    "v_exp_f32 v152, v152\n\t" \
    "v_rcp_f32 v144, v144\n\t" \
    "v_exp_f32 v153, v153\n\t" \
    "s_waitcnt vmcnt(2) lgkmcnt(3)\n\t" \
    "v_mfma_f32_32x32x16_bf16 v[184:199], v[200:203], v[68:71], 0\n\t" \
    "ds_read_b128 v[200:203], v16 offset:4096\n\t" \
    "v_rcp_f32 v145, v145\n\t" \
    "v_cvt_pk_bf16_f32 v228, v168, v169\n\t" \
    "v_cvt_pk_bf16_f32 v229, v170, v171\n\t" \
    "v_exp_f32 v154, v154\n\t" \
    "v_mfma_f32_32x32x16_bf16 v[184:199], v[204:207], v[76:79], v[184:199]\n\t" \
    "ds_read_b128 v[204:207], v17 offset:4096\n\t" \
    "v_rcp_f32 v146, v146\n\t" \
    "v_cvt_pk_bf16_f32 v230, v172, v173\n\t" \
    "v_cvt_pk_bf16_f32 v231, v174, v175\n\t" \
    "v_exp_f32 v155, v155\n\t" \
    "v_mfma_f32_32x32x16_bf16 v[184:199], v[208:211], v[84:87], v[184:199]\n\t" \
    "ds_read_b128 v[208:211], v18 offset:4096\n\t" \
    "v_rcp_f32 v147, v147\n\t" \
    "v_exp_f32 v156, v156\n\t" \
    "v_rcp_f32 v148, v148\n\t" \
    "v_mfma_f32_32x32x16_bf16 v[184:199], v[212:215], v[92:95], v[184:199]\n\t" \
    "ds_read_b128 v[212:215], v19 offset:4096\n\t" \
    "v_exp_f32 v157, v157\n\t" \
    "v_rcp_f32 v149, v149\n\t" \
    "v_exp_f32 v158, v158\n\t" \
    "v_mfma_f32_32x32x16_bf16 v[104:119], v[220:223], v[228:231], v[104:119]\n\t" \
    "v_rcp_f32 v150, v150\n\t" \
    "v_exp_f32 v159, v159\n\t" \
    "s_mov_b32 m0, s74\n\t" \
    "v_rcp_f32 v151, v151\n\t" \
    "s_setprio 0\n\t" \
    "v_add_f32 v152, 1.0, v152\n\t" \
    "v_mul_f32 v176, v144, v176\n\t" \
    "v_add_f32 v153, 1.0, v153\n\t" \
    "v_mul_f32 v177, v145, v177\n\t" \
    "v_add_f32 v154, 1.0, v154\n\t" \
    "v_mul_f32 v178, v146, v178\n\t" \
    "v_add_f32 v155, 1.0, v155\n\t" \
    "v_mul_f32 v179, v147, v179\n\t" \
    "v_add_f32 v156, 1.0, v156\n\t" \
    "v_mul_f32 v180, v148, v180\n\t" \
    "v_add_f32 v157, 1.0, v157\n\t" \
    "v_mul_f32 v181, v149, v181\n\t" \
    "v_add_f32 v158, 1.0, v158\n\t" \
    "v_mul_f32 v182, v150, v182\n\t" \
    "v_add_f32 v159, 1.0, v159\n\t" \
    "v_mul_f32 v183, v151, v183\n\t" \
    "buffer_load_dwordx4 v234, %[rx], 0 offen lds\n\t" \
    "s_setprio 1\n\t" \
    "v_exp_f32 v160, v160\n\t" \
    "v_rcp_f32 v152, v152\n\t" \
    "v_exp_f32 v161, v161\n\t" \
    "s_waitcnt lgkmcnt(0)\n\t" \
    "v_mfma_f32_32x32x16_bf16 v[136:151], v[200:203], v[32:35], 0\n\t" \
    "v_and_or_b32 v219, v236, v25, v219\n\t" \
    "ds_write_b128 v26, v[216:219]\n\t" \
    "v_rcp_f32 v153, v153\n\t" \
    "v_cvt_pk_bf16_f32 v228, v176, v177\n\t" \
    "v_cvt_pk_bf16_f32 v229, v178, v179\n\t" \
    "v_exp_f32 v162, v162\n\t" \
    "v_mfma_f32_32x32x16_bf16 v[136:151], v[204:207], v[40:43], v[136:151]\n\t" \
    "v_rcp_f32 v154, v154\n\t" \
    "v_cvt_pk_bf16_f32 v230, v180, v181\n\t" \
    "v_cvt_pk_bf16_f32 v231, v182, v183\n\t" \
    "v_exp_f32 v163, v163\n\t" \
    "v_mfma_f32_32x32x16_bf16 v[136:151], v[208:211], v[48:51], v[136:151]\n\t" \
    "v_rcp_f32 v155, v155\n\t" \
    "s_cmp_lt_u32 %[i], s80\n\t" \
    "v_exp_f32 v164, v164\n\t" \
    "s_cselect_b32 %[t1], %[i], %[nullrel]\n\t" \
    "v_rcp_f32 v156, v156\n\t" \
    "v_mfma_f32_32x32x16_bf16 v[104:119], v[224:227], v[228:231], v[104:119]\n\t" \
    "v_exp_f32 v165, v165\n\t" \
    "s_add_u32 %[i], %[i], 0x100\n\t" \
    "v_rcp_f32 v157, v157\n\t" \
    "v_exp_f32 v166, v166\n\t" \
    "v_mfma_f32_32x32x16_bf16 v[136:151], v[212:215], v[56:59], v[136:151]\n\t" \
    "v_rcp_f32 v158, v158\n\t" \
    "s_mov_b32 m0, s75\n\t" \
    "v_exp_f32 v167, v167\n\t" \
    "v_rcp_f32 v159, v159\n\t" \
    "s_setprio 0\n\t" \
    "v_add_f32 v160, 1.0, v160\n\t" \
    "v_mul_f32 v184, v152, v184\n\t" \
    "v_add_f32 v161, 1.0, v161\n\t" \
    "v_mul_f32 v185, v153, v185\n\t" \
    "v_mfma_f32_32x32x16_bf16 v[136:151], v[216:219], v[96:99], v[136:151]\n\t" \
    "v_add_f32 v162, 1.0, v162\n\t" \
    "v_mul_f32 v186, v154, v186\n\t" \
    "v_add_f32 v163, 1.0, v163\n\t" \
    "v_mul_f32 v187, v155, v187\n\t" \
    "v_add_f32 v164, 1.0, v164\n\t" \
    "v_mul_f32 v188, v156, v188\n\t" \
    "v_add_f32 v165, 1.0, v165\n\t" \
    "v_mul_f32 v189, v157, v189\n\t" \
    "v_add_f32 v166, 1.0, v166\n\t" \
    "v_mul_f32 v190, v158, v190\n\t" \
    "v_add_f32 v167, 1.0, v167\n\t" \
    "v_mul_f32 v191, v159, v191\n\t" \
    "v_add_u32 v238, %[t1], v20\n\t" \
    "buffer_load_dwordx4 v235, %[rx], 0 offen lds\n\t" \
    "s_setprio 1\n\t" \
    "v_exp_f32 v136, v136\n\t" \
    "ds_read2_b32 v[232:233], v238 offset0:0 offset1:16\n\t" \
    "v_rcp_f32 v160, v160\n\t" \
    "ds_read2_b32 v[234:235], v238 offset0:32 offset1:48\n\t" \
    "v_exp_f32 v137, v137\n\t" \
    "ds_read_b32 v236, v21 offset:512\n\t" \
    "v_mfma_f32_32x32x16_bf16 v[168:183], v[200:203], v[64:67], 0\n\t" \
    "v_rcp_f32 v161, v161\n\t" \
    "v_cvt_pk_bf16_f32 v228, v184, v185\n\t" \
    "v_cvt_pk_bf16_f32 v229, v186, v187\n\t" \
    "v_exp_f32 v138, v138\n\t" \
    "v_mfma_f32_32x32x16_bf16 v[168:183], v[204:207], v[72:75], v[168:183]\n\t" \
    "v_rcp_f32 v162, v162\n\t" \
    "v_cvt_pk_bf16_f32 v230, v188, v189\n\t" \
    "v_cvt_pk_bf16_f32 v231, v190, v191\n\t" \
    "v_exp_f32 v139, v139\n\t" \
    "v_mfma_f32_32x32x16_bf16 v[168:183], v[208:211], v[80:83], v[168:183]\n\t" \
    "v_rcp_f32 v163, v163\n\t" \
    "v_exp_f32 v140, v140\n\t" \
    "v_rcp_f32 v164, v164\n\t" \
    "v_mfma_f32_32x32x16_bf16 v[120:135], v[220:223], v[228:231], v[120:135]\n\t" \
    "v_exp_f32 v141, v141\n\t" \
    "ds_read_b64_tr_b16 v[220:221], v27 offset:0\n\t" \
    "v_rcp_f32 v165, v165\n\t" \
    "ds_read_b64_tr_b16 v[222:223], v27 offset:128\n\t" \
    "v_exp_f32 v142, v142\n\t" \
    "s_waitcnt lgkmcnt(2)\n\t" \
    "v_mfma_f32_32x32x16_bf16 v[168:183], v[212:215], v[88:91], v[168:183]\n\t" \
    "v_rcp_f32 v166, v166\n\t" \
    "v_exp_f32 v143, v143\n\t" \
    "s_mov_b32 m0, s76\n\t" \
    "v_rcp_f32 v167, v167\n\t" \
    "s_setprio 0\n\t" \
    "v_add_f32 v136, 1.0, v136\n\t" \
    "v_mul_f32 v192, v160, v192\n\t" \
    "v_add_f32 v137, 1.0, v137\n\t" \
    "v_mul_f32 v193, v161, v193\n\t" \
    "v_add_f32 v138, 1.0, v138\n\t" \
    "v_mul_f32 v194, v162, v194\n\t" \
    "v_add_f32 v139, 1.0, v139\n\t" \
    "v_mul_f32 v195, v163, v195\n\t" \
    "v_add_f32 v140, 1.0, v140\n\t" \
    "v_mul_f32 v196, v164, v196\n\t" \
    "v_add_f32 v141, 1.0, v141\n\t" \
    "v_mul_f32 v197, v165, v197\n\t" \
    "v_add_f32 v142, 1.0, v142\n\t" \
    "v_mul_f32 v198, v166, v198\n\t" \
    "v_add_f32 v143, 1.0, v143\n\t" \
    "v_mul_f32 v199, v167, v199\n\t" \
    "v_add_u32 v232, v232, v22\n\t" \
    "v_add_u32 v233, v233, v23\n\t" \
    "v_add_u32 v234, v234, v22\n\t" \
    "v_add_u32 v235, v235, v23\n\t" \
    "buffer_load_dwordx4 v232, %[rx], 0 offen lds\n\t" \
    "s_setprio 1\n\t" \
    "v_exp_f32 v144, v144\n\t" \
    "v_rcp_f32 v136, v136\n\t" \
    "v_exp_f32 v145, v145\n\t" \
    "v_mfma_f32_32x32x16_bf16 v[152:167], v[200:203], v[36:39], 0\n\t" \
    "v_rcp_f32 v137, v137\n\t" \
    "v_cvt_pk_bf16_f32 v228, v192, v193\n\t" \
    "v_cvt_pk_bf16_f32 v229, v194, v195\n\t" \
    "v_exp_f32 v146, v146\n\t" \
    "v_mfma_f32_32x32x16_bf16 v[152:167], v[204:207], v[44:47], v[152:167]\n\t" \
    "v_rcp_f32 v138, v138\n\t" \
    "v_cvt_pk_bf16_f32 v230, v196, v197\n\t" \
    "v_cvt_pk_bf16_f32 v231, v198, v199\n\t" \
    "v_exp_f32 v147, v147\n\t" \
    "v_mfma_f32_32x32x16_bf16 v[152:167], v[208:211], v[52:55], v[152:167]\n\t" \
    "v_rcp_f32 v139, v139\n\t" \
    "v_exp_f32 v148, v148\n\t" \
    "v_rcp_f32 v140, v140\n\t" \
    "v_mfma_f32_32x32x16_bf16 v[120:135], v[224:227], v[228:231], v[120:135]\n\t" \
    "v_exp_f32 v149, v149\n\t" \
    "ds_read_b64_tr_b16 v[224:225], v27 offset:256\n\t" \
    "v_rcp_f32 v141, v141\n\t" \
    "ds_read_b64_tr_b16 v[226:227], v27 offset:384\n\t" \
    "v_exp_f32 v150, v150\n\t" \
    "v_mfma_f32_32x32x16_bf16 v[152:167], v[212:215], v[60:63], v[152:167]\n\t" \
    "v_rcp_f32 v142, v142\n\t" \
    "s_cmp_ge_u32 %[i], s81\n\t" \
    "v_exp_f32 v151, v151\n\t" \
    "s_mov_b32 m0, s77\n\t" \
    "v_rcp_f32 v143, v143\n\t" \
    "s_setprio 0\n\t" \
    "v_add_f32 v144, 1.0, v144\n\t" \
    "v_mul_f32 v168, v136, v168\n\t" \
    "v_add_f32 v145, 1.0, v145\n\t" \
    "v_mul_f32 v169, v137, v169\n\t" \
    "v_mfma_f32_32x32x16_bf16 v[152:167], v[216:219], v[100:103], v[152:167]\n\t" \
    "v_add_f32 v146, 1.0, v146\n\t" \
    "v_mul_f32 v170, v138, v170\n\t" \
    "v_add_f32 v147, 1.0, v147\n\t" \
    "v_mul_f32 v171, v139, v171\n\t" \
    "v_add_f32 v148, 1.0, v148\n\t" \
    "v_mul_f32 v172, v140, v172\n\t" \
    "v_add_f32 v149, 1.0, v149\n\t" \
    "v_mul_f32 v173, v141, v173\n\t" \
    "v_add_f32 v150, 1.0, v150\n\t" \
    "v_mul_f32 v174, v142, v174\n\t" \
    "v_add_f32 v151, 1.0, v151\n\t" \
    "v_mul_f32 v175, v143, v175\n\t" \
    "buffer_load_dwordx4 v233, %[rx], 0 offen lds\n\t" \
    "s_setprio 1\n\t" \
    "s_cbranch_scc1 LAST%=\n\t" \
    "v_and_or_b32 v237, v236, %[m1e0], v24\n\t" \
    "ds_read_b128 v[216:219], v237 offset:%[lut]\n\t" \
    "v_exp_f32 v152, v152\n\t" \
    "v_rcp_f32 v144, v144\n\t" \
    "v_exp_f32 v153, v153\n\t" \
    "s_waitcnt vmcnt(2) lgkmcnt(3)\n\t" \
    "v_mfma_f32_32x32x16_bf16 v[184:199], v[200:203], v[68:71], 0\n\t" \
    "ds_read_b128 v[200:203], v16 offset:0\n\t" \
    "v_rcp_f32 v145, v145\n\t" \
    "v_cvt_pk_bf16_f32 v228, v168, v169\n\t" \
    "v_cvt_pk_bf16_f32 v229, v170, v171\n\t" \
    "v_exp_f32 v154, v154\n\t" \
    "v_mfma_f32_32x32x16_bf16 v[184:199], v[204:207], v[76:79], v[184:199]\n\t" \
    "ds_read_b128 v[204:207], v17 offset:0\n\t" \
    "v_rcp_f32 v146, v146\n\t" \
    "v_cvt_pk_bf16_f32 v230, v172, v173\n\t" \
    "v_cvt_pk_bf16_f32 v231, v174, v175\n\t" \
    "v_exp_f32 v155, v155\n\t" \
    "v_mfma_f32_32x32x16_bf16 v[184:199], v[208:211], v[84:87], v[184:199]\n\t" \
    "ds_read_b128 v[208:211], v18 offset:0\n\t" \
    "v_rcp_f32 v147, v147\n\t" \
    "v_exp_f32 v156, v156\n\t" \
    "v_rcp_f32 v148, v148\n\t" \
    "v_mfma_f32_32x32x16_bf16 v[184:199], v[212:215], v[92:95], v[184:199]\n\t" \
    "ds_read_b128 v[212:215], v19 offset:0\n\t" \
    "v_exp_f32 v157, v157\n\t" \
    "v_rcp_f32 v149, v149\n\t" \
    "v_exp_f32 v158, v158\n\t" \
    "v_mfma_f32_32x32x16_bf16 v[104:119], v[220:223], v[228:231], v[104:119]\n\t" \
    "v_rcp_f32 v150, v150\n\t" \
    "v_exp_f32 v159, v159\n\t" \
    "s_mov_b32 m0, s78\n\t" \
    "v_rcp_f32 v151, v151\n\t" \
    "s_setprio 0\n\t" \
    "v_add_f32 v152, 1.0, v152\n\t" \
    "v_mul_f32 v176, v144, v176\n\t" \
    "v_add_f32 v153, 1.0, v153\n\t" \
    "v_mul_f32 v177, v145, v177\n\t" \
    "v_add_f32 v154, 1.0, v154\n\t" \
    "v_mul_f32 v178, v146, v178\n\t" \
    "v_add_f32 v155, 1.0, v155\n\t" \
    "v_mul_f32 v179, v147, v179\n\t" \
    "v_add_f32 v156, 1.0, v156\n\t" \
    "v_mul_f32 v180, v148, v180\n\t" \
    "v_add_f32 v157, 1.0, v157\n\t" \
    "v_mul_f32 v181, v149, v181\n\t" \
    "v_add_f32 v158, 1.0, v158\n\t" \
    "v_mul_f32 v182, v150, v182\n\t" \
    "v_add_f32 v159, 1.0, v159\n\t" \
    "v_mul_f32 v183, v151, v183\n\t" \
    "buffer_load_dwordx4 v234, %[rx], 0 offen lds\n\t" \
    "s_setprio 1\n\t" \
    "v_exp_f32 v160, v160\n\t" \
    "v_rcp_f32 v152, v152\n\t" \
    "v_exp_f32 v161, v161\n\t" \
    "s_waitcnt lgkmcnt(0)\n\t" \
    "v_mfma_f32_32x32x16_bf16 v[136:151], v[200:203], v[32:35], 0\n\t" \
    "v_and_or_b32 v219, v236, v25, v219\n\t" \
    "ds_write_b128 v26, v[216:219]\n\t" \
    "v_rcp_f32 v153, v153\n\t" \
    "v_cvt_pk_bf16_f32 v228, v176, v177\n\t" \
    "v_cvt_pk_bf16_f32 v229, v178, v179\n\t" \
    "v_exp_f32 v162, v162\n\t" \
    "v_mfma_f32_32x32x16_bf16 v[136:151], v[204:207], v[40:43], v[136:151]\n\t" \
    "v_rcp_f32 v154, v154\n\t" \
    "v_cvt_pk_bf16_f32 v230, v180, v181\n\t" \
    "v_cvt_pk_bf16_f32 v231, v182, v183\n\t" \
    "v_exp_f32 v163, v163\n\t" \
    "v_mfma_f32_32x32x16_bf16 v[136:151], v[208:211], v[48:51], v[136:151]\n\t" \
    "v_rcp_f32 v155, v155\n\t" \
    "s_cmp_lt_u32 %[i], s80\n\t" \
    "v_exp_f32 v164, v164\n\t" \
    "s_cselect_b32 %[t1], %[i], %[nullrel]\n\t" \
    "v_rcp_f32 v156, v156\n\t" \
    "v_mfma_f32_32x32x16_bf16 v[104:119], v[224:227], v[228:231], v[104:119]\n\t" \
    "v_exp_f32 v165, v165\n\t" \
    "s_add_u32 %[i], %[i], 0x100\n\t" \
    "v_rcp_f32 v157, v157\n\t" \
    "v_exp_f32 v166, v166\n\t" \
    "v_mfma_f32_32x32x16_bf16 v[136:151], v[212:215], v[56:59], v[136:151]\n\t" \
    "v_rcp_f32 v158, v158\n\t" \
    "s_mov_b32 m0, s79\n\t" \
    "v_exp_f32 v167, v167\n\t" \
    "v_rcp_f32 v159, v159\n\t" \
    "s_setprio 0\n\t" \
    "v_add_f32 v160, 1.0, v160\n\t" \
    "v_mul_f32 v184, v152, v184\n\t" \
    "v_add_f32 v161, 1.0, v161\n\t" \
    "v_mul_f32 v185, v153, v185\n\t" \
    "v_mfma_f32_32x32x16_bf16 v[136:151], v[216:219], v[96:99], v[136:151]\n\t" \
    "v_add_f32 v162, 1.0, v162\n\t" \
    "v_mul_f32 v186, v154, v186\n\t" \
    "v_add_f32 v163, 1.0, v163\n\t" \
    "v_mul_f32 v187, v155, v187\n\t" \
    "v_add_f32 v164, 1.0, v164\n\t" \
    "v_mul_f32 v188, v156, v188\n\t" \
    "v_add_f32 v165, 1.0, v165\n\t" \
    "v_mul_f32 v189, v157, v189\n\t" \
    "v_add_f32 v166, 1.0, v166\n\t" \
    "v_mul_f32 v190, v158, v190\n\t" \
    "v_add_f32 v167, 1.0, v167\n\t" \
    "v_mul_f32 v191, v159, v191\n\t" \
    "v_add_u32 v238, %[t1], v20\n\t" \
    "v_add_u32 v21, 0x200, v21\n\t" \
    "buffer_load_dwordx4 v235, %[rx], 0 offen lds\n\t" \
    "s_setprio 1\n\t" \
    "s_branch LOOP%=\n\t" \
    "LAST%=:\n\t" \
    "v_exp_f32 v152, v152\n\t" \
    "v_rcp_f32 v144, v144\n\t" \
    "v_exp_f32 v153, v153\n\t" \
    "s_waitcnt lgkmcnt(2)\n\t" \
    "v_mfma_f32_32x32x16_bf16 v[184:199], v[200:203], v[68:71], 0\n\t" \
    "v_rcp_f32 v145, v145\n\t" \
    "v_cvt_pk_bf16_f32 v228, v168, v169\n\t" \
    "v_cvt_pk_bf16_f32 v229, v170, v171\n\t" \
    "v_exp_f32 v154, v154\n\t" \
    "v_mfma_f32_32x32x16_bf16 v[184:199], v[204:207], v[76:79], v[184:199]\n\t" \
    "v_rcp_f32 v146, v146\n\t" \
    "v_cvt_pk_bf16_f32 v230, v172, v173\n\t" \
    "v_cvt_pk_bf16_f32 v231, v174, v175\n\t" \
    "v_exp_f32 v155, v155\n\t" \
    "v_mfma_f32_32x32x16_bf16 v[184:199], v[208:211], v[84:87], v[184:199]\n\t" \
    "v_rcp_f32 v147, v147\n\t" \
    "v_exp_f32 v156, v156\n\t" \
    "v_rcp_f32 v148, v148\n\t" \
    "v_mfma_f32_32x32x16_bf16 v[104:119], v[220:223], v[228:231], v[104:119]\n\t" \
    "v_exp_f32 v157, v157\n\t" \
    "v_rcp_f32 v149, v149\n\t" \
    "v_exp_f32 v158, v158\n\t" \
    "v_mfma_f32_32x32x16_bf16 v[184:199], v[212:215], v[92:95], v[184:199]\n\t" \
    "v_rcp_f32 v150, v150\n\t" \
    "v_exp_f32 v159, v159\n\t" \
    "v_rcp_f32 v151, v151\n\t" \
    "s_setprio 0\n\t" \
    "v_add_f32 v152, 1.0, v152\n\t" \
    "v_mul_f32 v176, v144, v176\n\t" \
    "v_add_f32 v153, 1.0, v153\n\t" \
    "v_mul_f32 v177, v145, v177\n\t" \
    "v_add_f32 v154, 1.0, v154\n\t" \
    "v_mul_f32 v178, v146, v178\n\t" \
    "v_add_f32 v155, 1.0, v155\n\t" \
    "v_mul_f32 v179, v147, v179\n\t" \
    "v_add_f32 v156, 1.0, v156\n\t" \
    "v_mul_f32 v180, v148, v180\n\t" \
    "v_add_f32 v157, 1.0, v157\n\t" \
    "v_mul_f32 v181, v149, v181\n\t" \
    "v_add_f32 v158, 1.0, v158\n\t" \
    "v_mul_f32 v182, v150, v182\n\t" \
    "v_add_f32 v159, 1.0, v159\n\t" \
    "v_mul_f32 v183, v151, v183\n\t" \
    "s_setprio 1\n\t" \
    "v_exp_f32 v160, v160\n\t" \
    "v_rcp_f32 v152, v152\n\t" \
    "v_exp_f32 v161, v161\n\t" \
    "s_waitcnt lgkmcnt(0)\n\t" \
    "v_rcp_f32 v153, v153\n\t" \
    "v_cvt_pk_bf16_f32 v228, v176, v177\n\t" \
    "v_cvt_pk_bf16_f32 v229, v178, v179\n\t" \
    "v_exp_f32 v162, v162\n\t" \
    "v_rcp_f32 v154, v154\n\t" \
    "v_cvt_pk_bf16_f32 v230, v180, v181\n\t" \
    "v_cvt_pk_bf16_f32 v231, v182, v183\n\t" \
    "v_exp_f32 v163, v163\n\t" \
    "v_rcp_f32 v155, v155\n\t" \
    "v_exp_f32 v164, v164\n\t" \
    "v_rcp_f32 v156, v156\n\t" \
    "v_mfma_f32_32x32x16_bf16 v[104:119], v[224:227], v[228:231], v[104:119]\n\t" \
    "v_exp_f32 v165, v165\n\t" \
    "v_rcp_f32 v157, v157\n\t" \
    "v_exp_f32 v166, v166\n\t" \
    "v_rcp_f32 v158, v158\n\t" \
    "v_exp_f32 v167, v167\n\t" \
    "v_rcp_f32 v159, v159\n\t" \
    "s_setprio 0\n\t" \
    "v_add_f32 v160, 1.0, v160\n\t" \
    "v_mul_f32 v184, v152, v184\n\t" \
    "v_add_f32 v161, 1.0, v161\n\t" \
    "v_mul_f32 v185, v153, v185\n\t" \
    "v_add_f32 v162, 1.0, v162\n\t" \
    "v_mul_f32 v186, v154, v186\n\t" \
    "v_add_f32 v163, 1.0, v163\n\t" \
    "v_mul_f32 v187, v155, v187\n\t" \
    "v_add_f32 v164, 1.0, v164\n\t" \
    "v_mul_f32 v188, v156, v188\n\t" \
    "v_add_f32 v165, 1.0, v165\n\t" \
    "v_mul_f32 v189, v157, v189\n\t" \
    "v_add_f32 v166, 1.0, v166\n\t" \
    "v_mul_f32 v190, v158, v190\n\t" \
    "v_add_f32 v167, 1.0, v167\n\t" \
    "v_mul_f32 v191, v159, v191\n\t" \
    "s_setprio 1\n\t" \
    "DONE%=:\n\t" \
    "v_rcp_f32 v160, v160\n\t" \
    "v_rcp_f32 v161, v161\n\t" \
    "v_rcp_f32 v162, v162\n\t" \
    "v_rcp_f32 v163, v163\n\t" \
    "v_rcp_f32 v164, v164\n\t" \
    "v_rcp_f32 v165, v165\n\t" \
    "v_rcp_f32 v166, v166\n\t" \
    "v_rcp_f32 v167, v167\n\t" \
    "v_cvt_pk_bf16_f32 v228, v184, v185\n\t" \
    "v_cvt_pk_bf16_f32 v229, v186, v187\n\t" \
    "v_cvt_pk_bf16_f32 v230, v188, v189\n\t" \
    "v_cvt_pk_bf16_f32 v231, v190, v191\n\t" \
    "s_nop 1\n\t" \
    "v_mfma_f32_32x32x16_bf16 v[120:135], v[220:223], v[228:231], v[120:135]\n\t" \
    "v_mul_f32 v192, v160, v192\n\t" \
    "v_mul_f32 v193, v161, v193\n\t" \
    "v_mul_f32 v194, v162, v194\n\t" \
    "v_mul_f32 v195, v163, v195\n\t" \
    "v_mul_f32 v196, v164, v196\n\t" \
    "v_mul_f32 v197, v165, v197\n\t" \
    "v_mul_f32 v198, v166, v198\n\t" \
    "v_mul_f32 v199, v167, v199\n\t" \
    "v_cvt_pk_bf16_f32 v228, v192, v193\n\t" \
    "v_cvt_pk_bf16_f32 v229, v194, v195\n\t" \
    "v_cvt_pk_bf16_f32 v230, v196, v197\n\t" \
    "v_cvt_pk_bf16_f32 v231, v198, v199\n\t" \
    "s_nop 1\n\t" \
    "v_mfma_f32_32x32x16_bf16 v[120:135], v[224:227], v[228:231], v[120:135]\n\t" \
    "END%=:\n\t" \
    "s_waitcnt vmcnt(0)\n\t" \
    "s_waitcnt lgkmcnt(0)\n\t" \
    "s_setprio 0\n\t" \
    "s_nop 7\n\t" \
    "s_nop 7\n\t" \
    "s_branch OUT%=\n\t" \
    "NOTFIRST%=:\n\t" \
    "s_waitcnt lgkmcnt(0)\n\t" \
    "ds_read_b128 v[216:219], v237 offset:%[lut]\n\t" \
    "ds_read_b128 v[136:139], v29 offset:16384\n\t" \
    "ds_read_b128 v[140:143], v29 offset:17408\n\t" \
    "ds_read_b128 v[144:147], v29 offset:18432\n\t" \
    "ds_read_b128 v[148:151], v29 offset:19456\n\t" \
    "ds_read_b128 v[152:155], v29 offset:20480\n\t" \
    "ds_read_b128 v[156:159], v29 offset:21504\n\t" \
    "ds_read_b128 v[160:163], v29 offset:22528\n\t" \
    "ds_read_b128 v[164:167], v29 offset:23552\n\t" \
    "s_waitcnt lgkmcnt(0)\n\t" \
    "ds_read_b128 v[32:35], v29 offset:0\n\t" \
    "ds_read_b128 v[36:39], v29 offset:1024\n\t" \
    "ds_read_b128 v[40:43], v29 offset:2048\n\t" \
    "ds_read_b128 v[44:47], v29 offset:3072\n\t" \
    "ds_read_b128 v[48:51], v29 offset:4096\n\t" \
    "ds_read_b128 v[52:55], v29 offset:5120\n\t" \
    "ds_read_b128 v[56:59], v29 offset:6144\n\t" \
    "ds_read_b128 v[60:63], v29 offset:7168\n\t" \
    "v_mfma_f32_32x32x16_bf16 v[168:183], v[200:203], v[136:139], 0\n\t" \
    "v_mfma_f32_32x32x16_bf16 v[184:199], v[200:203], v[140:143], 0\n\t" \
    "v_mfma_f32_32x32x16_bf16 v[168:183], v[204:207], v[144:147], v[168:183]\n\t" \
    "v_mfma_f32_32x32x16_bf16 v[184:199], v[204:207], v[148:151], v[184:199]\n\t" \
    "v_mfma_f32_32x32x16_bf16 v[168:183], v[208:211], v[152:155], v[168:183]\n\t" \
    "v_mfma_f32_32x32x16_bf16 v[184:199], v[208:211], v[156:159], v[184:199]\n\t" \
    "v_mfma_f32_32x32x16_bf16 v[168:183], v[212:215], v[160:163], v[168:183]\n\t" \
    "v_mfma_f32_32x32x16_bf16 v[184:199], v[212:215], v[164:167], v[184:199]\n\t" \
    "s_waitcnt lgkmcnt(0)\n\t" \
    "s_branch JOIN%=\n\t" \
    "OUT%=:\n\t"
#define K2_LOOP_CLOBBERS "v32", "v33", "v34", "v35", "v36", "v37", "v38", "v39", "v40", "v41", "v42", "v43", "v44", "v45", "v46", "v47", "v48", "v49", "v50", "v51", "v52", "v53", "v54", "v55", "v56", "v57", "v58", "v59", "v60", "v61", "v62", "v63", "v64", "v65", "v66", "v67", "v68", "v69", "v70", "v71", "v72", "v73", "v74", "v75", "v76", "v77", "v78", "v79", "v80", "v81", "v82", "v83", "v84", "v85", "v86", "v87", "v88", "v89", "v90", "v91", "v92", "v93", "v94", "v95", "v96", "v97", "v98", "v99", "v100", "v101", "v102", "v103", "v136", "v137", "v138", "v139", "v140", "v141", "v142", "v143", "v144", "v145", "v146", "v147", "v148", "v149", "v150", "v151", "v152", "v153", "v154", "v155", "v156", "v157", "v158", "v159", "v160", "v161", "v162", "v163", "v164", "v165", "v166", "v167", "v168", "v169", "v170", "v171", "v172", "v173", "v174", "v175", "v176", "v177", "v178", "v179", "v180", "v181", "v182", "v183", "v184", "v185", "v186", "v187", "v188", "v189", "v190", "v191", "v192", "v193", "v194", "v195", "v196", "v197", "v198", "v199", "v200", "v201", "v202", "v203", "v204", "v205", "v206", "v207", "v208", "v209", "v210", "v211", "v212", "v213", "v214", "v215", "v216", "v217", "v218", "v219", "v220", "v221", "v222", "v223", "v224", "v225", "v226", "v227", "v228", "v229", "v230", "v231", "v232", "v233", "v234", "v235", "v236", "v237", "v238", "s72", "s73", "s74", "s75", "s76", "s77", "s78", "s79", "s80", "s81", "vcc"


__device__ __forceinline__ void dma16(__amdgpu_buffer_rsrc_t rsrc, u32 voff, u32 ldsbase) {
  asm volatile("s_mov_b32 m0, %2\n\ts_nop 0\n\tbuffer_load_dwordx4 %0, %1, 0 offen lds"
               :: "v"(voff), "s"(rsrc), "s"(ldsbase) : "memory");
}
}
extern "C" __global__ __launch_bounds__(512, 2) void egc_k2_main(char* __restrict__ ws, const u16* __restrict__ x,
                                                  const u16* __restrict__ gw, u16* __restrict__ out) {
  __shared__ __attribute__((aligned(1024))) char lds[L_TOTAL];
  const int tid = threadIdx.x, l = tid & 63;
  const int b = (blockIdx.x & 7) * 32 + (blockIdx.x >> 3);
  const int v = __builtin_amdgcn_readfirstlane(tid >> 6);
  const __amdgpu_buffer_rsrc_t rx = __builtin_amdgcn_make_buffer_rsrc((void*)x, 0, NN * 128, 0x00020000);
  const __amdgpu_buffer_rsrc_t rw = __builtin_amdgcn_make_buffer_rsrc((void*)(ws + WS_WF), 0, 32768, 0x00020000);
  const __amdgpu_buffer_rsrc_t rr = __builtin_amdgcn_make_buffer_rsrc((void*)(ws + WS_REC), 0, (u32)WS_REC_BYTES, 0x00020000);
  const __amdgpu_buffer_rsrc_t ro = __builtin_amdgcn_make_buffer_rsrc((void*)(ws + WS_OFFS), 0, (u32)WS_OFFS_BYTES, 0x00020000);
  const __amdgpu_buffer_rsrc_t rg = __builtin_amdgcn_make_buffer_rsrc((void*)(gw + 128 * 64), 0, 128, 0x00020000);
  const __amdgpu_buffer_rsrc_t rout = __builtin_amdgcn_make_buffer_rsrc((void*)out, 0, NN * 128, 0x00020000);
  const int r = l & 31, h = l >> 5;
  const int gg = l >> 3, c8 = l & 7;
  const u32 swz0 = (u32)((c8 ^ (gg >> 1)) << 4), swz1 = swz0 ^ 64u;
  const u32 ringa = L_RING + v * 8192;
  const int tseg = 32 * v + r;
  u32x4 o; u32 o8, cg0, cg1;
  {
    const u32 oa = (u32)(tseg * OFFS_STRIDE + 8 * b) * 2u;
    asm volatile("s_nop 4\n\tbuffer_load_dwordx4 %0, %2, %3, 0 offen\n\tbuffer_load_ushort %1, %2, %3, 0 offen offset:16"
                 : "=&v"(o), "=&v"(o8) : "v"(oa), "s"(ro) : "memory");
  }
  asm volatile("s_barrier" ::: "memory");
#pragma unroll
  for (int i = 0; i < 4; ++i) dma16(rw, (u32)((4 * v + i) * 1024 + l * 16), L_WST + (4 * v + i) * 1024);
  asm volatile("buffer_load_ushort %0, %2, %3, 0 offen\n\tbuffer_load_ushort %1, %2, %3, 0 offen offset:64"
               : "=&v"(cg0), "=&v"(cg1) : "v"((u32)(r * 2)), "s"(rg) : "memory");
  if (tid < 32) {
    const u32 d = tid >> 1, hh = tid & 1;
    const u32 j = (((d >> 2) & 1u) == hh && d != 15u) ? ((d & 3u) | ((d >> 3) << 2)) : 8u;
    u32x4 q;
#pragma unroll
    for (u32 j2 = 0; j2 < 4; ++j2) q[j2] = (j >> 1) == j2 ? (0x3f80u << ((j & 1u) * 16u)) : 0u;
    *(u32x4*)(lds + L_LUT + tid * 16) = q;
    *(u32x2*)(lds + L_NULL + tid * 8) = u32x2{NULL_W0, NULL_W1};
    *(u32x4*)(lds + L_ZERO + tid * 16) = u32x4{0, 0, 0, 0};
  }
  u32 tb8[4];
#pragma unroll
  for (int rho = 0; rho < 4; ++rho) tb8[rho] = (u32)((32 * v + 8 * rho + gg) * CH + 2 * c8) * 8u;
  asm volatile("s_waitcnt vmcnt(6)" : "+v"(o), "+v"(o8) :: "memory");
  const u32 start = o[0] & 0xffffu, len = o8 - start;
  const bool lng = l < 32 && len > 16u;
  const u64 lmask = __builtin_amdgcn_ballot_w64(lng);
  const u32 lrank = __builtin_amdgcn_mbcnt_lo((u32)lmask, 0u);
  const u32 sl = start | (len << 12) | ((lng && lrank < 16u) ? (1u << 24) : 0u);
  const u32 slt = (u32)__builtin_amdgcn_ds_permute((int)((lng ? lrank : 63u) * 4u), (int)(start | (len << 12) | ((u32)r << 24)));
  u32 qq[4];
#pragma unroll
  for (int rho = 0; rho < 4; ++rho) qq[rho] = (u32)__builtin_amdgcn_ds_bpermute((8 * rho + gg) * 4, (int)sl);
  const u32 nlong = (u32)__builtin_popcountll(lmask);
  u32 qt[2];
  qt[0] = (u32)__builtin_amdgcn_ds_bpermute(gg * 4, (int)slt);
  qt[1] = (u32)__builtin_amdgcn_ds_bpermute((8 + gg) * 4, (int)slt);
  u32 cn[4], sc[4];
  cn[0] = __builtin_amdgcn_alignbit(o[1], o[0], 16) - o[0];
  cn[1] = __builtin_amdgcn_alignbit(o[2], o[1], 16) - o[1];
  cn[2] = __builtin_amdgcn_alignbit(o[3], o[2], 16) - o[2];
  cn[3] = __builtin_amdgcn_alignbit(o8, o[3], 16) - o[3];
  u32x4 ra[4], rb[2]; u32 i0[4], it[2], tbt[2]; int rem[4], remt[2];
  auto head = [&](int rho) {
    i0[rho] = (qq[rho] & 0xfffu) + 2 * c8;
    rem[rho] = (int)((qq[rho] >> 12) & 0xfffu) - 2 * c8;
    const u32 aa = rem[rho] > 0 ? tb8[rho] + (qq[rho] & 0xfffu) * 8u : 0xffffff00u;
    asm volatile("buffer_load_dwordx4 %0, %1, %2, 0 offen" : "=&v"(ra[rho]) : "v"(aa), "s"(rr) : "memory");
  };
  auto tail = [&](int k) {
    const u32 seg = 32 * v + (qt[k] >> 24);
    tbt[k] = seg * 16;
    it[k] = (qt[k] & 0xfffu) + 16 + 2 * c8;
    remt[k] = 8 * k + gg < (int)nlong ? (int)((qt[k] >> 12) & 0xfffu) - 16 - 2 * c8 : 0;
    const u32 aa = remt[k] > 0 ? (seg * CH + it[k]) * 8u : 0xffffff00u;
    asm volatile("buffer_load_dwordx4 %0, %1, %2, 0 offen" : "=&v"(rb[k]) : "v"(aa), "s"(rr) : "memory");
  };
  head(0); head(1); head(2); head(3); tail(0);
  rb[1] = u32x4{0, 0, 0, 0}; remt[1] = 0; it[1] = 0; tbt[1] = 0;
  if (__builtin_expect(nlong > 8u, 0)) tail(1);
#pragma unroll
  for (int j = 0; j < 4; ++j) sc[j] = dpp_add<0x111, 0xf>(cn[j]);
#pragma unroll
  for (int j = 0; j < 4; ++j) sc[j] = dpp_add<0x112, 0xf>(sc[j]);
#pragma unroll
  for (int j = 0; j < 4; ++j) sc[j] = dpp_add<0x114, 0xf>(sc[j]);
#pragma unroll
  for (int j = 0; j < 4; ++j) sc[j] = dpp_add<0x118, 0xf>(sc[j]);
#pragma unroll
  for (int j = 0; j < 4; ++j) sc[j] = dpp_add<0x142, 0xa>(sc[j]);
  asm volatile("" : "+v"(sc[0]), "+v"(sc[1]), "+v"(sc[2]), "+v"(sc[3]));
  const u32 big = __builtin_amdgcn_ballot_w64(len > 255u) != 0;
  if (l == 31) {
    *(u32x4*)(lds + L_WTOT + v * 16) = u32x4{sc[0], sc[1], sc[2], sc[3]};
    *(u32*)(lds + L_WTOT + 128 + v * 4) = big;
  }
  asm volatile("s_waitcnt lgkmcnt(0)\n\ts_barrier" ::: "memory");
  {
    const int nv = win_start(8 * b + v);
#pragma unroll
    for (int i = 0; i < 2; ++i) dma16(rx, (u32)(nv + 8 * i + gg) * 128u + (i ? swz1 : swz0), L_OWN + v * 2048 + i * 1024);
  }
  const int wi = l >> 3, wj = l & 7;
  const u32 scr = L_SCR + v * 1152;
  u32 Tj, Bj, total; int wsel; bool anybig;
  {
    const u32 cw = *(const u16*)(lds + L_WTOT + l * 2);
    const u32 bq = *(const u32*)(lds + L_WTOT + 128 + wj * 4);
    auto red = [](u32 x) {
      x = dpp_add<0x128, 0xf>(x);
      const auto p = __builtin_amdgcn_permlane16_swap(x, x, false, false); x = p[0] + p[1];
      const auto q = __builtin_amdgcn_permlane32_swap(x, x, false, false); return q[0] + q[1];
    };
    Tj = red(cw);
    const u32 Pj = red(wi < v ? cw : 0u);
    const u32 Rj = (Tj + 31u) & ~31u;
    u32 inc = dpp_add<0x111, 0xf>(Rj); inc = dpp_add<0x112, 0xf>(inc); inc = dpp_add<0x114, 0xf>(inc);
    Bj = inc - Rj;
    if (l < 8) *(u16*)(lds + scr + 2 * l) = (u16)(Bj + Pj + DBIAS);
    const u32 key = (Tj << 3) | (7u - wj);
    const u32 kw = (u32)__builtin_amdgcn_ds_bpermute(wi * 4, (int)key);
    asm volatile("" ::: "memory");
    const u32x4 Ep = *(const u32x4*)(lds + scr);
    total = (u32)__builtin_amdgcn_readlane((int)inc, 7);
    anybig = __builtin_amdgcn_ballot_w64(bq != 0) != 0 || total > (u32)CAPREC;
    if (l < 32 && anybig) *(u32*)(lds + L_D + tseg * 4) = start | (len << 16);
    if (l < 32 && !anybig) {
      u32x4 D;
#pragma unroll
      for (int j = 0; j < 4; ++j) {
        typedef unsigned short u16x2 __attribute__((ext_vector_type(2)));
        const u32 ej = Ep[j], oj = o[j];
        const u16x2 aa = __builtin_bit_cast(u16x2, ej + (sc[j] - cn[j])), bb = __builtin_bit_cast(u16x2, oj);
        D[j] = __builtin_bit_cast(u32, (u16x2)(aa - bb));
      }
      *(u32x4*)(lds + L_D + tseg * 16) = D;
    }
    const u64 gt = __builtin_amdgcn_ballot_w64(key > kw);
    const u32 rank = __builtin_popcount((u32)(gt >> (l & 56)) & 0xffu);
    const u32 want = v < 4 ? v : 11 - v;
    wsel = (int)(__builtin_ctzll(__builtin_amdgcn_ballot_w64(rank == want)) >> 3);
    if (anybig) wsel = v;
  }
  u32 Tv = (u32)__builtin_amdgcn_readlane((int)Tj, wsel), Bv = (u32)__builtin_amdgcn_readlane((int)Bj, wsel);
  const int n0 = win_start(8 * b + wsel), nn = win_start(8 * b + wsel + 1) - n0;
  if (!anybig && l < (int)((0u - Tv) & 31u)) *(u32x2*)(lds + L_LIST + (Bv + Tv + l) * 8) = u32x2{NULL_W0, NULL_W1};
  asm volatile("s_waitcnt vmcnt(2)"
               : "+v"(ra[0]), "+v"(ra[1]), "+v"(ra[2]), "+v"(ra[3]), "+v"(rb[0]), "+v"(rb[1]), "+v"(cg0), "+v"(cg1) :: "memory");
  if (__builtin_expect(!anybig, 1)) {
    constexpr int LOFF = L_LIST - (int)DBIAS * 8;
    u32 dd[5][2];
#pragma unroll
    for (int rho = 0; rho < 4; ++rho) {
      const u32 tb = (32 * v + 8 * rho + gg) * 16;
      dd[rho][0] = *(const u16*)(lds + L_D + (tb | (ra[rho][1] & 0xeu)));
      dd[rho][1] = *(const u16*)(lds + L_D + (tb | (ra[rho][3] & 0xeu)));
    }
    dd[4][0] = *(const u16*)(lds + L_D + (tbt[0] | (rb[0][1] & 0xeu)));
    dd[4][1] = *(const u16*)(lds + L_D + (tbt[0] | (rb[0][3] & 0xeu)));
#pragma unroll
    for (int rho = 0; rho < 5; ++rho) asm volatile("" : "+v"(dd[rho][0]), "+v"(dd[rho][1]));
#pragma unroll
    for (int rho = 0; rho < 4; ++rho) {
      if (rem[rho] > 0) *(u32x2*)(lds + LOFF + ((dd[rho][0] + i0[rho]) << 3)) = u32x2{ra[rho][0], ra[rho][1]};
      if (rem[rho] > 1) *(u32x2*)(lds + LOFF + 8 + ((dd[rho][1] + i0[rho]) << 3)) = u32x2{ra[rho][2], ra[rho][3]};
    }
    if (remt[0] > 0) *(u32x2*)(lds + LOFF + ((dd[4][0] + it[0]) << 3)) = u32x2{rb[0][0], rb[0][1]};
    if (remt[0] > 1) *(u32x2*)(lds + LOFF + 8 + ((dd[4][1] + it[0]) << 3)) = u32x2{rb[0][2], rb[0][3]};
    if (__builtin_expect(nlong > 8u, 0)) {
      const u32 d0 = *(const u16*)(lds + L_D + (tbt[1] | (rb[1][1] & 0xeu)));
      const u32 d1 = *(const u16*)(lds + L_D + (tbt[1] | (rb[1][3] & 0xeu)));
      if (remt[1] > 0) *(u32x2*)(lds + LOFF + ((d0 + it[1]) << 3)) = u32x2{rb[1][0], rb[1][1]};
      if (remt[1] > 1) *(u32x2*)(lds + LOFF + 8 + ((d1 + it[1]) << 3)) = u32x2{rb[1][2], rb[1][3]};
    }
    if (__builtin_expect(nlong > 16u || __builtin_amdgcn_ballot_w64(l < 32 && len > 32u) != 0, 0)) {
#pragma unroll 1
      for (int rho = 0; rho < 4; ++rho) {
        const u32 q = rho == 0 ? qq[0] : (rho == 1 ? qq[1] : (rho == 2 ? qq[2] : qq[3]));
        const u32 t = 32 * v + 8 * rho + gg, s0 = q & 0xfffu;
        const int ln = (int)((q >> 12) & 0xfffu), from = (q >> 24) ? 32 : 16;
        for (int base = 0; __builtin_amdgcn_ballot_w64(ln > from + base); base += 8) {
          const int rel = from + base + c8;
          if (rel < ln) {
            const u32x2 rc = *(const u32x2*)(ws + WS_REC + ((size_t)t * CH + s0 + rel) * 8);
            const u32 dq = *(const u16*)(lds + L_D + ((t * 16) | (rc[1] & 0xeu)));
            *(u32x2*)(lds + LOFF + ((dq + s0 + rel) << 3)) = rc;
          }
        }
      }
    }
  }
  const u32 cp0 = pack_bf16(0.f, bf2f(cg0) * NLOG2E), cp1 = pack_bf16(0.f, bf2f(cg1) * NLOG2E);
  f32x32 Yv;
  asm volatile("" : "={v[104:135]}"(Yv));
  u32 tnext = 0, first = 1;
  u32 ng;
  do {
    if (__builtin_expect(anybig, 0)) {
      int tid = threadIdx.x;
      asm volatile("" : "+v"(tid));
      const int l = tid & 63;
      const u32* seg = (const u32*)(lds + L_D);
      int* tab = (int*)(lds + L_RING);
      u32* tw = (u32*)(lds + L_RING + 8192);
      __syncthreads();
      u32 t1 = tnext, tot = 0;
      while (t1 < (u32)NCH) {
        const u32 ln = (u32)__builtin_amdgcn_readfirstlane((int)(seg[t1] >> 16));
        if (tot + ln > (u32)CAPREC - 8u * 31u) break;
        tot += ln; ++t1;
      }
      const u16* offs = (const u16*)(ws + WS_OFFS) + 8 * b + v;
      u32 of[4], cn4[4];
#pragma unroll
      for (int k = 0; k < 4; ++k) {
        const u32 t = 4 * l + k;
        of[k] = offs[t * OFFS_STRIDE];
        cn4[k] = (t >= tnext && t < t1) ? offs[t * OFFS_STRIDE + 1] - of[k] : 0u;
      }
      const u32 loc = cn4[0] + cn4[1] + cn4[2] + cn4[3], incl = scan64(loc);
      Tv = (u32)__builtin_amdgcn_readlane((int)incl, 63);
      if (l == 0) tw[v] = Tv;
      __syncthreads();
      const u32 Rq = (tw[l & 7] + 31u) & ~31u;
      u32 inc = dpp_add<0x111, 0xf>(Rq); inc = dpp_add<0x112, 0xf>(inc); inc = dpp_add<0x114, 0xf>(inc);
      Bv = (u32)__builtin_amdgcn_readlane((int)(inc - Rq), v);
      u32 ex = Bv + incl - loc;
#pragma unroll
      for (int k = 0; k < 4; ++k) { tab[(4 * l + k) * 8 + v] = (int)(ex - of[k]); ex += cn4[k]; }
      if (l < (int)((0u - Tv) & 31u)) *(u32x2*)(lds + L_LIST + (Bv + Tv + l) * 8) = u32x2{NULL_W0, NULL_W1};
      __syncthreads();
      const u32x2* rec = (const u32x2*)(ws + WS_REC);
      for (u32 t = tnext + v; t < t1; t += 8) {
        const u32 sg = seg[t], s0 = sg & 0xffffu;
        for (u32 i = l; i < (sg >> 16); i += 64) {
          const u32x2 q = rec[(size_t)t * CH + s0 + i];
          *(u32x2*)(lds + L_LIST + (tab[t * 8 + ((q[1] >> 1) & 7u)] + (int)(s0 + i)) * 8) = q;
        }
      }
      tnext = t1;
    }
  asm volatile("s_waitcnt vmcnt(0) lgkmcnt(0)\n\ts_barrier" ::: "memory");
  ng = (Tv + 31u) >> 5;
  {
    int tq = tid;
    asm volatile("" : "+v"(tq));
    const int l2 = tq & 63, r2 = l2 & 31, h2 = l2 >> 5, g2 = l2 >> 3;
    const u32 sw2 = (u32)(((l2 & 7) ^ (g2 >> 1)) << 4);
    const u32 listb = L_LIST + Bv * 8;
    const u32 aoff = r2 * 128 + ((h2 ^ ((r2 >> 1) & 7)) << 4);
    const int p4 = l2 & 3, q4 = (l2 >> 2) & 3;
    u32x16 ADv = {ringa + aoff, ringa + (aoff ^ 32u), ringa + (aoff ^ 64u), ringa + (aoff ^ 96u),
                  listb + g2 * 8, listb + r2 * 8 + 4, sw2, sw2 ^ 64u,
                  (u32)h2 * 16u, h2 ? 0xffff0000u : 0u, scr + h2 * 576 + r2 * 16,
                  (l2 & 16) ? (u32)L_ZERO : scr + (p4 & 1) * 576 + (p4 >> 1) * 8 + (4 * h2 + q4) * 16,
                  (u32)(L_OWN + wsel * 2048) - ringa, (u32)(L_WST + l2 * 16), cp0, cp1};
    u32 si, t0, t1;
    asm volatile(K2_LOOP_ASM
                 : "+{v[104:135]}"(Yv), "+{v[16:31]}"(ADv), [i] "=&s"(si), [t0] "=&s"(t0), [t1] "=&s"(t1)
                 : [rx] "s"(rx), [ring] "s"(ringa), [ng] "s"(ng),
                   [nullrel] "s"((u32)L_NULL - listb), [m1e0] "s"(0x1e0u), [lut] "i"(L_LUT), [nl2e] "s"(NLOG2E), [first] "s"(first), [zero] "i"(L_ZERO)
                 : K2_LOOP_CLOBBERS, "memory", "scc");
  }
    first = 0;
  } while (__builtin_expect(anybig, 0) && tnext < (u32)NCH);
  int te = tid;
  asm volatile("" : "+v"(te));
  const int le = te & 63, re = le & 31, he = le >> 5;
  u16* stg = (u16*)(lds + ringa);
#pragma unroll
  for (int t = 0; t < 2; ++t)
#pragma unroll
    for (int j = 0; j < 8; ++j) {
      const float y = Yv[16 * t + j];
      const float s = y * __builtin_amdgcn_rcpf(1.0f + __builtin_amdgcn_exp2f(y * NLOG2E));
      const int row = (j & 3) + 8 * (j >> 2) + 4 * he;
      stg[row * 64 + 32 * t + re] = (u16)pack_bf16(s, s);
    }
  asm volatile("" ::: "memory");
#pragma unroll
  for (int i = 0; i < 2; ++i) {
    const int ch = le + 64 * i;
    const u32x4 q = *(const u32x4*)((const char*)stg + ch * 16);
    __builtin_amdgcn_raw_buffer_store_b128(q, rout, (ch >> 3) < nn ? (u32)(n0 * 128 + ch * 16) : 0xffffff00u, 0, AUX_SC1);
  }
}

extern "C" void kernel_launch(void* const* d_in, const int* in_sizes, int n_in, void* d_out, int out_size,
                              void* d_ws, size_t ws_size, hipStream_t stream) {
  const u16* x = (const u16*)d_in[0];
  const u16* ea = (const u16*)d_in[1];
  const int* src = (const int*)d_in[2];
  const int* dst = (const int*)d_in[3];
  const u16* uw = (const u16*)d_in[4];
  const u16* vw = (const u16*)d_in[5];
  const u16* gw = (const u16*)d_in[6];
  k1_bucket<<<NCH, 320, 0, stream>>>(src, dst, ea, gw, vw, uw, x, (const u16*)d_out, (char*)d_ws);
  egc_k2_main<<<NCH, 512, 0, stream>>>((char*)d_ws, x, gw, (u16*)d_out);
}
```
